```python
import torch

def _softmax(x, axis=-1):
    return torch.softmax(x, dim=axis)

def _gelu(x):
    return torch.nn.functional.gelu(x, approximate='tanh')

def _silu(x):
    return torch.nn.functional.silu(x)

def _sigmoid(x):
    return torch.sigmoid(x)

def _layernorm(x, axis=-1, eps=1e-05):
    m = x.mean(dim=axis, keepdim=True)
    v = x.var(dim=axis, keepdim=True, unbiased=False)
    return (x - m) / torch.sqrt(v + eps)

def _scatter_add(tgt, idx, src, axis=0):
    return tgt.clone().index_add_(axis, idx, src)

def _segment_sum(src, seg, n):
    out = torch.zeros((n,) + src.shape[1:], dtype=src.dtype)
    return out.index_add_(0, seg, src)

def _pad(x, pw):
    flat = tuple((v for pair in reversed(pw) for v in pair))
    return torch.nn.functional.pad(x, flat)
kernel_name = 'interleave_layers__memory_aug_recurrent_mem_tr__dense_activation_gelu__prompt'

def setup_inputs():

    _w = lambda a: (lambda t: t.to(torch.bfloat16) if t.is_floating_point() else t)(torch.as_tensor(a))

    def _A_setup():
        B, S, Dm = (1, 2048, 512)
        H, Dh = (32, 128)
        M, seg = (8, 32)
        seg = min(seg, S)
        S = S // seg * seg
        return {'x': _w((lambda *s: torch.randn(*s, dtype=torch.bfloat16) * 0.05)(B, S, Dm)), 'mem0': _w((lambda *s: torch.randn(*s, dtype=torch.bfloat16) * 0.2)(B, M, Dm)), 'Wq': _w((lambda *s: torch.randn(*s, dtype=torch.bfloat16) * 1.6)(Dm, H, Dh) * Dm ** (-0.5)), 'Wk': _w((lambda *s: torch.randn(*s, dtype=torch.bfloat16) * 1.6)(Dm, H, Dh) * Dm ** (-0.5)), 'Wv': _w((lambda *s: torch.randn(*s, dtype=torch.bfloat16) * 0.2)(Dm, H, Dh) * Dm ** (-0.5)), 'Wo': _w((lambda *s: torch.randn(*s, dtype=torch.bfloat16) * 0.2)(H, Dh, Dm) * (H * Dh) ** (-0.5)), 'seg_len': seg}
    _A_in = _A_setup()

    def _B_setup():
        B, S, D, F = (1, 2048, 512, 1376)
        return {'x': _w((lambda *s: torch.randn(*s, dtype=torch.bfloat16) * 0.05)(B, S, D)), 'w1': _w((lambda *s: torch.randn(*s, dtype=torch.bfloat16) * 0.4)(D, F) * D ** (-0.5)), 'b1': _w((lambda *s: torch.randn(*s, dtype=torch.bfloat16) * 12.8)(F) * 0.01), 'w2': _w((lambda *s: torch.randn(*s, dtype=torch.bfloat16) * 0.1)(F, D) * F ** (-0.5)), 'b2': _w((lambda *s: torch.randn(*s, dtype=torch.bfloat16) * 0.8)(D) * 0.01)}
    _B_in = _B_setup()
    return {'A_x': _A_in['x'], 'A_mem0': _A_in['mem0'], 'A_Wq': _A_in['Wq'], 'A_Wk': _A_in['Wk'], 'A_Wv': _A_in['Wv'], 'A_Wo': _A_in['Wo'], 'A_seg_len': _A_in['seg_len'], 'B_x': _B_in['x'], 'B_w1': _B_in['w1'], 'B_b1': _B_in['b1'], 'B_w2': _B_in['w2'], 'B_b2': _B_in['b2']}

def reference(A_x, A_mem0, A_Wq, A_Wk, A_Wv, A_Wo, A_seg_len, B_x, B_w1, B_b1, B_w2, B_b2):

    def _A_ref(x, mem0, Wq, Wk, Wv, Wo, seg_len):
        B, S, Dm = x.shape
        H, Dh = (Wq.shape[1], Wq.shape[2])
        M = mem0.shape[1]
        scale = Dh ** (-0.5)
        n_seg = S // seg_len
        L = M + seg_len
        idx = torch.arange(L)
        mask = (idx[:, None] < M) | (idx[:, None] >= idx[None, :])
        mem = mem0
        outs = []
        for s in range(n_seg):
            xs = x[:, s * seg_len:(s + 1) * seg_len, :].clone()
            h = torch.concatenate([mem, xs], axis=1)
            q = torch.einsum('btd,dhe->bhte', h, Wq)
            k = torch.einsum('btd,dhe->bhte', h, Wk)
            v = torch.einsum('btd,dhe->bhte', h, Wv)
            scores = torch.einsum('bhqe,bhke->bhqk', q, k) * scale
            scores = torch.where(mask[None, None], scores, -1000000000.0)
            p = _softmax(scores, axis=-1)
            o = torch.einsum('bhqk,bhke->bhqe', p, v)
            o = torch.einsum('bhte,hed->btd', o, Wo)
            mem = o[:, :M, :].clone()
            outs.append(o[:, M:, :])
        return torch.concatenate(outs, axis=1)

    def _B_ref(x, w1, b1, w2, b2):
        h = torch.einsum('bsd,df->bsf', x, w1) + b1
        h = _gelu(h)
        return torch.einsum('bsf,fd->bsd', h, w2) + b2
    x = A_x
    x = x + _A_ref(_layernorm(x, axis=-1), A_mem0, A_Wq, A_Wk, A_Wv, A_Wo, A_seg_len)
    x = x + _B_ref(_layernorm(x, axis=-1), B_w1, B_b1, B_w2, B_b2)
    x = x + _A_ref(_layernorm(x, axis=-1), A_mem0, A_Wq, A_Wk, A_Wv, A_Wo, A_seg_len)
    x = x + _B_ref(_layernorm(x, axis=-1), B_w1, B_b1, B_w2, B_b2)
    return x
```

```cpp
#include <hip/hip_runtime.h>
#include <hip/hip_bf16.h>
#include <stdint.h>

#define DBG_STAMP 0

namespace rmt {

typedef __bf16 bf16;
typedef __bf16 bf16x2 __attribute__((ext_vector_type(2)));
typedef __bf16 bf16x4 __attribute__((ext_vector_type(4)));
typedef __bf16 bf16x8 __attribute__((ext_vector_type(8)));
typedef __bf16 bf16x16 __attribute__((ext_vector_type(16)));
typedef short s16x4 __attribute__((ext_vector_type(4)));
typedef float f32x2 __attribute__((ext_vector_type(2)));
typedef float f32x4 __attribute__((ext_vector_type(4)));
typedef unsigned u32;
typedef unsigned u32x2 __attribute__((ext_vector_type(2)));
typedef unsigned u32x4 __attribute__((ext_vector_type(4)));
typedef unsigned u32x8 __attribute__((ext_vector_type(8)));
typedef unsigned long long u64;
#define LAS __attribute__((address_space(3)))
#define GAS __attribute__((address_space(1)))
#define DEVI __device__ __forceinline__

constexpr int DM = 512, NH = 32, DH = 128, NM = 8, SEG = 32, NSEG = 64, SEQ = 2048, FF = 1376;
constexpr int NFB = FF / 32;
constexpr int NFC = 11;
constexpr u32 KB = 1024;
constexpr u32 WBYTES = DM * NH * DH * 2, FBYTES = DM * FF * 2;
constexpr u32 SENT = 0xffffffffu;
constexpr u32 OOB = 0xf0000000u;

constexpr u32 MB = 1024 * KB;
constexpr u32 OFF_FLAGS = 0;
constexpr u32 FLAGS_BYTES = 128 * KB;
constexpr u32 ZONE_BYTES = 2 * MB, ZONE_INBOX = 1024 * KB, ZONE_FKV = 1536 * KB, ZONE_USED = ZONE_FKV + 80 * KB, OFF_ZONE = 2 * MB;
constexpr u32 OFF_X0 = 8 * MB;
constexpr u32 OFF_H = 16 * MB;
constexpr u32 OFF_X1 = 24 * MB;
#define HSL 0
#define LSHM 4
#define H1M 196
constexpr u32 OFF_H1 = H1M * MB;
constexpr u32 OFF_KV = 64 * MB;
constexpr u32 OFF_PF = 128 * MB;
constexpr u32 OFF_OX = 32 * MB;
constexpr u32 OFF_KVM = OFF_OX;
constexpr u32 OFF_DBG = 210 * MB + 128 * KB;
constexpr u32 OFF_PO = 212 * MB;
constexpr u32 LSH = LSHM * MB;
constexpr u32 WS_USED = 248 * MB;
__host__ __device__ constexpr u32 sp(u32 o) { return (o >> 13 << 14) | (o & 8191); }
__device__ __forceinline__ u32 flip(u32 o) { return o ^ ((o >> 8) & 0x2000); }

constexpr u32 F_ABORT = 0;
constexpr u32 OFF_GO = FLAGS_BYTES;
constexpr u32 OFF_DONE = OFF_GO + KB;
constexpr u32 NPROBE = 8;
constexpr u32 F_O = 8 * KB;
constexpr u32 F_PO = F_O + 192 * 128;
constexpr u32 F_PF = F_PO + 32 * 256;
constexpr u32 F_X1 = F_PF + 32 * 256;
constexpr u32 F_H = F_X1 + 32 * 256;
static_assert(sp(F_H + 24 * 256) + 8 * KB <= FLAGS_BYTES && ZONE_USED + NPROBE * 16 * KB <= ZONE_BYTES, "flags");

constexpr u32 SPIN_CAP = 1u << 20;

struct Params {
  const bf16* x; const bf16* mem0; const bf16* wq; const bf16* wk; const bf16* wv; const bf16* wo;
  const bf16* w1; const bf16* b1; const bf16* w2; const bf16* b2;
  bf16* out; char* ws;
};

__host__ __device__ constexpr int sigma(int a, int g, int j) { return 32 * a + 16 * (j >> 2) + 4 * g + (j & 3); }
__host__ __device__ constexpr int pi(int a, int g, int j) { return 128 * (a >> 2) + 64 * (j >> 2) + 16 * (a & 3) + 4 * (j & 3) + g; }
__device__ __forceinline__ char* ws_base(char* ws) { return ws + ((0u - (unsigned)(uintptr_t)ws) & (2 * MB - 1)); }

DEVI u32 pack2(float lo, float hi) { bf16x2 v; v[0] = (bf16)lo; v[1] = (bf16)hi; return __builtin_bit_cast(u32, v); }
DEVI float bf_lo(u32 v) { return __builtin_bit_cast(float, v << 16); }
DEVI float bf_hi(u32 v) { return __builtin_bit_cast(float, v & 0xffff0000u); }
DEVI u32 f2u(float f) { return __builtin_bit_cast(u32, f); }
DEVI float u2f(u32 u) { return __builtin_bit_cast(float, u); }
DEVI bf16x8 as_bf8(u32x4 v) { return __builtin_bit_cast(bf16x8, v); }
DEVI bf16x16 cat16(u32x4 a, u32x4 b) {
  u32x8 r = __builtin_shufflevector(a, b, 0, 1, 2, 3, 4, 5, 6, 7); return __builtin_bit_cast(bf16x16, r); }
DEVI f32x4 mfma32(bf16x8 a, bf16x8 b, f32x4 c) { return __builtin_amdgcn_mfma_f32_16x16x32_bf16(a, b, c, 0, 0, 0); }
DEVI f32x4 mfma16(u32x2 a, u32x2 b, f32x4 c) {
  return mfma32(as_bf8((u32x4){a.x, a.y, 0u, 0u}), as_bf8((u32x4){b.x, b.y, 0u, 0u}), c); }
DEVI f32x4 smfmac(u32x4 a, bf16x16 b, f32x4 c, int idx) {
  return __builtin_amdgcn_smfmac_f32_16x16x64_bf16(as_bf8(a), b, c, idx, 0, 0); }

DEVI float xor32_max(float x) { auto r = __builtin_amdgcn_permlane32_swap(f2u(x), f2u(x), false, false); return fmaxf(u2f(r[0]), u2f(r[1])); }
DEVI float xor16_max(float x) { auto r = __builtin_amdgcn_permlane16_swap(f2u(x), f2u(x), false, false); return fmaxf(u2f(r[0]), u2f(r[1])); }
DEVI float xor32_add(float x) { auto r = __builtin_amdgcn_permlane32_swap(f2u(x), f2u(x), false, false); return u2f(r[0]) + u2f(r[1]); }
DEVI float xor16_add(float x) { auto r = __builtin_amdgcn_permlane16_swap(f2u(x), f2u(x), false, false); return u2f(r[0]) + u2f(r[1]); }
DEVI float fold32(float a, float b) { auto r = __builtin_amdgcn_permlane32_swap(f2u(a), f2u(b), false, false); return u2f(r[0]) + u2f(r[1]); }
DEVI float fold16(float a, float b) { auto r = __builtin_amdgcn_permlane16_swap(f2u(a), f2u(b), false, false); return u2f(r[0]) + u2f(r[1]); }
template <int CTRL> DEVI float dpp_mov(float x) { return u2f(__builtin_amdgcn_update_dpp(0u, f2u(x), CTRL, 0xf, 0xf, true)); }
DEVI float xor8_add(float x) { return x + dpp_mov<0x128>(x); }
DEVI float xor1_get(float x) { return dpp_mov<0xB1>(x); }

typedef __amdgpu_buffer_rsrc_t rsrc_t;
DEVI rsrc_t make_rsrc(const void* p, u32 bytes) { return __builtin_amdgcn_make_buffer_rsrc((void*)p, 0, bytes, 0x00020000); }
DEVI u32x4 ld16(rsrc_t r, u32 off) { return __builtin_amdgcn_raw_buffer_load_b128(r, off, 0, 0); }
DEVI u32x4 ld16c(rsrc_t r, u32 off) { return __builtin_amdgcn_raw_buffer_load_b128(r, off, 0, 16); }
DEVI u32x2 ld8c(rsrc_t r, u32 off) { return __builtin_amdgcn_raw_buffer_load_b64(r, off, 0, 16); }
DEVI u32 ld4c(rsrc_t r, u32 off) { return __builtin_amdgcn_raw_buffer_load_b32(r, off, 0, 16); }
DEVI void st16c(rsrc_t r, u32 off, u32x4 v) { __builtin_amdgcn_raw_buffer_store_b128(v, r, off, 0, 16); }
DEVI void st8c(rsrc_t r, u32 off, u32x2 v) { __builtin_amdgcn_raw_buffer_store_b64(v, r, off, 0, 16); }
DEVI void st4c(rsrc_t r, u32 off, u32 v) { __builtin_amdgcn_raw_buffer_store_b32(v, r, off, 0, 16); }

DEVI u32x4 make_srd(const void* p, u32 bytes) {
  u64 a = (u64)p; u32x4 r;
  r.x = __builtin_amdgcn_readfirstlane((u32)a); r.y = __builtin_amdgcn_readfirstlane((u32)(a >> 32)); r.z = bytes; r.w = 0x00020000; return r; }
DEVI void dma16(u32x4 srd, u32 lds_base, u32 voff) {
  u32 keep;
  asm volatile("s_mov_b32 %0, m0\n\ts_mov_b32 m0, %1\n\ts_nop 0\n\tbuffer_load_dwordx4 %2, %3, 0 offen sc1 lds\n\ts_mov_b32 m0, %0"
               : "=&s"(keep) : "s"(lds_base), "v"(voff), "s"(srd) : "memory");
}
DEVI void vm_wait0() { asm volatile("s_waitcnt vmcnt(0)" ::: "memory"); }
template <int N> DEVI void vm_wait() { asm volatile("s_waitcnt vmcnt(%0)" :: "n"(N) : "memory"); }
DEVI void wg_barrier() { asm volatile("s_waitcnt lgkmcnt(0)\n\ts_barrier" ::: "memory"); }
DEVI u32x4 lds_ld16(u32 a) { return *(LAS u32x4*)a; }
DEVI u32x2 lds_ld8(u32 a) { return *(LAS u32x2*)a; }
DEVI void lds_st16(u32 a, u32x4 v) { *(LAS u32x4*)a = v; }
DEVI void lds_st8(u32 a, u32x2 v) { *(LAS u32x2*)a = v; }
DEVI void lds_st2(u32 a, bf16 v) { *(LAS bf16*)a = v; }
typedef LAS char* lptr;
DEVI lptr lp(u32 a) { asm volatile("" : "+v"(a)); return (lptr)a; }
DEVI u32x4 lds_ld16(lptr b, int o) { return *(LAS u32x4*)(b + o); }
DEVI void lds_st16(lptr b, int o, u32x4 v) { *(LAS u32x4*)(b + o) = v; }
DEVI void lds_st8(lptr b, int o, u32x2 v) { *(LAS u32x2*)(b + o) = v; }
DEVI void lds_st2(lptr b, int o, bf16 v) { *(LAS bf16*)(b + o) = v; }
DEVI bool has_sent(u32x4 v) { return (v.x == SENT) | (v.y == SENT) | (v.z == SENT) | (v.w == SENT); }
DEVI u32x4 sel4(bool c, u32x4 a, u32x4 b) { u32x4 r; r.x = c ? a.x : b.x; r.y = c ? a.y : b.y; r.z = c ? a.z : b.z; r.w = c ? a.w : b.w; return r; }
DEVI u64 clk() { u64 t; asm volatile("s_memtime %0\n\ts_waitcnt lgkmcnt(0)" : "=s"(t) :: "memory"); return t; }

#define LEADER 255
#define NSAMP 8
DEVI u32x2 tr4(u32x2 v) {
  auto r = __builtin_amdgcn_permlane32_swap(v.x, v.y, false, false);
  auto q = __builtin_amdgcn_permlane16_swap(__builtin_amdgcn_perm(r[1], r[0], 0x05040100), __builtin_amdgcn_perm(r[1], r[0], 0x07060302), false, false);
  return (u32x2){__builtin_amdgcn_perm(q[1], q[0], 0x05040100), __builtin_amdgcn_perm(q[1], q[0], 0x07060302)};
}
DEVI u32 setup(const Params& p, const u32 j) {
  const int tid = threadIdx.x, l = tid & 63, w = tid >> 6, c = l & 15, g = l >> 4;
  const rsrc_t rs = make_rsrc(p.ws, WS_USED);
  LAS float* red = (LAS float*)0;
  if (j < NPROBE) {
    const u32 po = OFF_ZONE + (j & 1) * ZONE_BYTES + ZONE_USED + j * 16 * KB + w * 2 * KB;
    st16c(rs, po + (l & 32) * 256 + ((l >> 3) & 3) * 384 + (l & 7) * 16, (u32x4){0u, 0u, 0u, 0u}); vm_wait0();
    u32 best[2] = {0xffffu, 0xffffu};
#pragma unroll
    for (int i = 0; i < NSAMP; ++i) {
      const u64 t0 = clk(); u32 v = ld4c(rs, po + ((i ^ w) & 1) * 8 * KB + (i >> 1) * 384); asm volatile("s_waitcnt vmcnt(0)" :: "v"(v) : "memory"); const u64 t1 = clk();
      best[i & 1] = min(best[i & 1], (u32)(t1 - t0));
    }
    LAS u32* sh = (LAS u32*)0;
    if (l == 0) { sh[2 * w + (w & 1)] = best[0]; sh[2 * w + (~w & 1)] = best[1]; }
    wg_barrier();
    return min(min(sh[0], sh[2]), min(sh[4], sh[6])) | min(min(sh[1], sh[3]), min(sh[5], sh[7])) << 16;
  }
  const u32 u = j - NPROBE, wu = __builtin_amdgcn_readfirstlane(w);
  if (u < NSEG) {
    const u32x4 srd = make_srd(p.x, SEQ * DM * 2);
#pragma unroll
    for (int t = 0; t < 8; ++t)
      asm volatile("s_mov_b32 m0, %0\n\ts_nop 0\n\tbuffer_load_dwordx4 %1, %2, %3 offen lds" :: "s"(64 * KB + (wu * 8 + t) * 1040), "v"(l * 16), "s"(srd), "s"((u * 32 + wu * 8 + t) * KB) : "memory");
  }
  constexpr u32 FILL = FLAGS_BYTES + ZONE_USED - 16 * KB, NFILL = (FILL + 320 * KB - 1) / (320 * KB) * 2;
#pragma unroll
  for (u32 e = 0; e < NFILL / 2; ++e) {
    const u32 o = (u + e * 80) * 4 * KB + tid * 16;
    const bool fl = o < FLAGS_BYTES;
    const u32 zo = o - FLAGS_BYTES + 16 * KB, v = fl || zo >= ZONE_FKV ? 0u : SENT;
    __builtin_amdgcn_raw_buffer_store_b128((u32x4){v, v, v, v}, rs, fl ? o : o < FILL ? OFF_ZONE + zo : OOB, 0, 16);
    __builtin_amdgcn_raw_buffer_store_b128((u32x4){v, v, v, v}, rs, fl || o >= FILL ? OOB : OFF_ZONE + ZONE_BYTES + zo, 0, 16);
  }
#define SST(i)
  if (u < NSEG) {
    u32x4 xv[2][4]; float mean[2];
    vm_wait<NFILL>(); wg_barrier();
    SST(3);
#pragma unroll
    for (int m = 0; m < 2; ++m) {
      float s = 0.f;
#pragma unroll
      for (int i = 0; i < 4; ++i) {
        const u32 a = 64 * KB + (m * 16 + c) * 1040 + 256 * w + 32 * i + 8 * g;
        const u32x2 lo = tr4(lds_ld8(a)), hi = tr4(lds_ld8(a + 128));
        xv[m][i] = (u32x4){lo.x, lo.y, hi.x, hi.y};
#pragma unroll
        for (int k = 0; k < 4; ++k) s += bf_lo(xv[m][i][k]) + bf_hi(xv[m][i][k]);
      }
      s = xor32_add(xor16_add(s));
      if (g == 0) red[(m * 4 + w) * 16 + c] = s;
    }
    wg_barrier();
    SST(1);
#pragma unroll
    for (int m = 0; m < 2; ++m) {
      mean[m] = (red[m * 64 + c] + red[m * 64 + 16 + c] + red[m * 64 + 32 + c] + red[m * 64 + 48 + c]) * (1.f / DM);
      float q = 0.f;
#pragma unroll
      for (int i = 0; i < 4; ++i)
#pragma unroll
        for (int k = 0; k < 4; ++k) { float a = bf_lo(xv[m][i][k]) - mean[m], b = bf_hi(xv[m][i][k]) - mean[m]; q += a * a + b * b; }
      q = xor32_add(xor16_add(q));
      if (g == 0) red[128 + (m * 4 + w) * 16 + c] = q;
    }
    wg_barrier();
#pragma unroll
    for (int m = 0; m < 2; ++m) {
      const float var = (red[128 + m * 64 + c] + red[128 + m * 64 + 16 + c] + red[128 + m * 64 + 32 + c] + red[128 + m * 64 + 48 + c]) * (1.f / DM);
      const float rstd = 1.f / sqrtf(var + 1e-5f);
#pragma unroll
      for (int i = 0; i < 4; ++i) {
        u32x4 hv;
#pragma unroll
        for (int k = 0; k < 4; ++k) hv[k] = pack2((bf_lo(xv[m][i][k]) - mean[m]) * rstd, (bf_hi(xv[m][i][k]) - mean[m]) * rstd);
        lds_st16((1 + m * 16 + c) * KB + (((4 * w + i) * 4 + g) ^ c) * 16, hv);
      }
    }
    wg_barrier();
    const u32 o = OFF_H + flip(sp((u * 32 + w * 8) * KB) + (w & 1) * 8 * KB) + l * 16;
#pragma unroll
    for (int t = 0; t < 8; ++t) st16c(rs, o + t * KB, lds_ld16((1 + w * 8 + t) * KB + l * 16));
    SST(2);
  } else if (u < NSEG + 2) {
    const int f = (u - NSEG) * 256 + tid, kap = f >> 6, hf = c & 1;
    const unsigned short* src = (const unsigned short*)p.mem0 + (c >> 1) * DM;
    u32x4 v;
#pragma unroll
    for (int e2 = 0; e2 < 4; ++e2) {
      const int d = 64 * kap + 32 * (g >> 1) + 16 * (e2 & 1) + 8 * (g & 1) + 2 * hf + (e2 >> 1);
      v[e2] = min((u32)src[d] | ((u32)src[d + 4] << 16), 0xfffffffeu);
    }
#pragma unroll
    for (int i = 0; i < 4; ++i) st16c(rs, OFF_ZONE + (i >> 1) * ZONE_BYTES + (i & 1) * 8 * KB + f * 16, v);
  }
  vm_wait0(); wg_barrier();
  return 0xffffffffu;
}

constexpr u32 LDS_BYTES = 160 * KB;
#define CHX 2
#define CH_L1D 0
#define CH_DEC 1
#define CH_CAP 8
#define CH_P2 52
#define CH_FM 16384
#define KVX 8192
#define HX 0
#define CH_P0 140
#define CH_E1 4
#define CH_B1MAX 44
#define CH_D1L 28
#define CH_DLATE 6
#define CH_D1MIN 24
#define CH_D1F 32
#define CH_D2F 28
#define CH_TOL 96
#define DBGT ".set DBGT, 0\n"
#define WNT 0
#define WPOL ""
#define STR2(x) #x
#define STR(x) STR2(x)
#define KERNEL_SYM "_ZN3rmt11main_kernelENS_6ParamsE"
#define ASM_TOP "memory", "vcc", "s99", "v255", "a255"

DEVI int lane_id() { int l; asm volatile("v_mbcnt_lo_u32_b32 %0, -1, 0\n\tv_mbcnt_hi_u32_b32 %0, -1, %0" : "=v"(l)); return l; }
DEVI u32 sh_col(int w, int c, int g) { return (c ^ (8 * (w & 1) + 2 * g)) * 16; }
DEVI u32 sh_rd(int w, int c, int g) { return (g + 1) * 32 * KB + (c >> 2) * 256 + (((4 * w) ^ (8 * (g & 1) + 2 * (c >> 2))) * 16 + (c & 3) * 8); }
DEVI void chain_role(const Params& p, const int w, const int L, const int h, const u32x8 go) {
  const u32 zb = OFF_ZONE + L * ZONE_BYTES, fb = 0;
  const int lane = lane_id();
  const int c = lane & 15, g = lane >> 4;
  const u32x4 srd = make_srd(p.ws, WS_USED);
  const u32 s_memr = zb + w * 2 * KB, s_memw = zb + 16 * KB + (h * 4 + w) * 64;
  const u32 s_inw = zb + ZONE_INBOX + w * 128 * KB + h * 64, s_inr = zb + ZONE_INBOX + h * 16 * KB + w * 2 * KB;
  const u32 s_kv = OFF_KV + sp((h * 4 + w) * 4 * KB) + (fb ^ KVX);
  const u32 s_kvm = OFF_KVM + sp(((u32)L * 64 * 32 + h) * 4 * KB) + (fb ^ 8 * KB);
  const u32 s_fkv = zb + ZONE_FKV + h * 128 + w * 4;
  const u32 s_wq = h * 256, s_wo = h * 128 * KB + w * 256;
  const u32x4 wl = {(u32)(8 * (w >> 1) + 4 * (g & 1) + (g & 2) + (w & 1)) * 8192 + sh_col(w, c, g), sh_rd(w, c, g),
                    g * KB + c * 16, (4 * (g & 1) + (g >> 1)) * KB + (c & 4) * 64 + (c & 2) * 32 + (c & 1) * 16 + (c & 8)};
  const u32 s_dbg = OFF_DBG + ((L * 32 + h) * 4 + w) * 64;
  const u32 q_wr = 0x6000 + c * 16 + g * 4, q_rd = 0x6000 + (4 * g + (c >> 2)) * 16 + 8 * (c & 1);
  const u32 k_wr = 0x6800 + g * 128 + ((c * 8) ^ ((g >> 1) * 64)), k_rd = 0x6800 + (c & 3) * 128 + (((4 * g + (c >> 2)) * 8) ^ ((c & 2) * 32));
  const u32 o_wr = w * 512 + g * 128 + (c & 7) * 16 + (c >> 3) * 8, o_rd = 0x6000 + w * 512 + g * 128 + (c >> 1) * 16;
  const u32 idx = (c & 1) ? 0xEEEE : 0x4444;
  const u32 one = (c & 2) ? 0u : ((c & 1) ? 0x3f800000u : 0x00003f80u);
  u32x4 asel = {(c >> 2) == 0 ? one : 0u, (c >> 2) == 1 ? one : 0u, (c >> 2) == 2 ? one : 0u, (c >> 2) == 3 ? one : 0u};
  u32x4 cst = {(c >> 2) * 16 * KB + g * 2 * KB + (c & 3) * 16, c < 4 ? (u32)((g >> 1) * 32 + c * 8 + (g & 1) * 4) : OOB, q_wr, k_wr};
  u32 a25 = s_memr, a26 = s_memw, a30 = s_kv, a31 = s_kvm, a33 = s_fkv, a24 = w, a40 = s_wq, a41 = s_wo;
  asm volatile(DBGT ".set CH_DEC, " STR(CH_DEC) "\n" ".set CH_CAP, " STR(CH_CAP) "\n" ".set CH_L1D, " STR(CH_L1D) "\n" ".set CH_FM, " STR(CH_FM) "\n" ".set CH_P0, " STR(CH_P0) "\n" ".set CH_P2, " STR(CH_P2) "\n" ".set CH_E1, " STR(CH_E1) "\n" ".set CH_B1MAX, " STR(CH_B1MAX) "\n" ".set CH_D1L, " STR(CH_D1L) "\n" ".set CH_DLATE, " STR(CH_DLATE) "\n" ".set CH_D1MIN, " STR(CH_D1MIN) "\n" ".set CH_D1F, " STR(CH_D1F) "\n" ".set CH_D2F, " STR(CH_D2F) "\n" ".set CH_TOL, " STR(CH_TOL) "\n" R"ASM(
  s_lshl_b32 s45, s24, 11
  s_lshl_b32 s46, s24, 12
  s_lshl_b32 s47, s24, 9
  s_mov_b32 s34, 0xff00ff00
  s_mov_b32 s35, 0xff00ff00
  s_mov_b32 s36, 0x00ff00ff
  s_mov_b32 s37, 0x00ff00ff
  s_mov_b32 s50, 0x3f803f80
  s_mov_b32 s51, 0x3f803f80
  s_mov_b32 s4, 0x3f003f00
  s_mov_b32 s5, 0x3f003f00
  s_mov_b32 s44, 0x3e0293ee
  s_mov_b32 s38, 0
  s_mov_b32 s42, 0
  s_mov_b32 s29, 0
  s_mov_b32 s48, 0
  s_lshl_b32 s52, s24, 15
  s_add_u32 s52, s52, 32768
  v_add_u32 v121, s52, v120
  ds_write_b128 v120, v[0:3] offset:30720
  ds_write_b128 v120, v[4:7] offset:31744
  s_lshl_b32 s6, s24, 10
  v_add_u32 v127, s6, v7
  ds_write_b32 v127, v8 offset:4
  ds_write_b32 v127, v9 offset:516
  s_add_u32 s31, s31, s47
  s_mov_b32 s11, CH_P0
  s_xor_b32 s69, s68, s24
  v_xor_b32 v15, 32, v11
  v_add_u32 v13, s52, v13
  v_mov_b32 v18, s12
  buffer_load_dword v18, off, s[20:23], s13 sc1
  s_mov_b32 s76, 3
  s_mov_b32 s77, 24576
  s_getpc_b64 s[74:75]
.Lch_pc_%=:
  s_add_u32 s74, s74, .Lch_ck_%=-.Lch_pc_%=
  s_addc_u32 s75, s75, 0
.Lch_dsub_%=:
  s_cmp_ge_u32 s76, 16
  s_cbranch_scc1 .Lch_dskip_%=
  s_mov_b32 s78, s76
  s_lshr_b32 m0, s78, 1
  s_and_b32 m0, m0, 6
  s_cmp_ge_u32 s78, 12
  s_cselect_b32 s53, 15, 20
  s_movrels_b64 s[56:57], s[60:61]
  s_cselect_b64 vcc, -1, 0
  s_cselect_b32 s43, s41, s40
  s_cselect_b32 s39, s24, s68
  s_cselect_b32 s79, 3, 127
  v_cndmask_b32 v14, v10, v12, vcc
  s_xor_b32 s39, s39, s78
  s_and_b32 s39, s39, 3
  s_lshl_b32 s39, s39, s53
  s_add_u32 s39, s39, s43
  s_lshl_b32 s78, s79, 10
  s_add_u32 m0, s52, s77
  s_waitcnt vmcnt(18) lgkmcnt(0)
  s_barrier
  s_cmp_lg_u32 s76, 5
  s_cbranch_scc1 .Lch_dgo_%=
  s_add_u32 s15, s15, 16
  v_mov_b32 v116, s15
  v_mov_b32 v117, -1
  s_mov_b64 exec, 1
  buffer_store_dwordx2 v[116:117], off, s[20:23], s14 sc1
  s_mov_b64 exec, -1
.Lch_dgo_%=:
  buffer_load_dword v18, off, s[20:23], s13 sc1
  buffer_load_dwordx4 v14, s[56:59], s39 offen offset:0 )ASM" WPOL R"ASM( lds
  s_add_u32 s39, s39, s78
  buffer_load_dwordx4 v14, s[56:59], s39 offen offset:1024 )ASM" WPOL R"ASM( lds
  s_add_u32 s39, s39, s78
  buffer_load_dwordx4 v14, s[56:59], s39 offen offset:2048 )ASM" WPOL R"ASM( lds
  s_add_u32 s39, s39, s78
  buffer_load_dwordx4 v14, s[56:59], s39 offen offset:3072 )ASM" WPOL R"ASM( lds
  s_add_u32 m0, m0, 4096
  s_add_u32 s39, s39, 4096
  s_add_u32 s39, s39, s78
  buffer_load_dwordx4 v14, s[56:59], s39 offen offset:0 )ASM" WPOL R"ASM( lds
  s_add_u32 s39, s39, s78
  buffer_load_dwordx4 v14, s[56:59], s39 offen offset:1024 )ASM" WPOL R"ASM( lds
  s_add_u32 s39, s39, s78
  buffer_load_dwordx4 v14, s[56:59], s39 offen offset:2048 )ASM" WPOL R"ASM( lds
  s_add_u32 s39, s39, s78
  buffer_load_dwordx4 v14, s[56:59], s39 offen offset:3072 )ASM" WPOL R"ASM( lds
.Lch_dnext_%=:
  s_add_u32 s77, s77, 8192
  s_and_b32 s77, s77, 24576
  s_add_u32 s76, s76, 1
  s_sub_u32 s78, s76, 4
  s_cbranch_scc1 .Lch_dsub_%=
  v_add_u32 v16, s77, v11
  v_add_u32 v17, s77, v15
  s_cmp_ge_u32 s78, 12
  s_cbranch_scc1 .Lch_dexit_%=
  s_xor_b32 s78, s78, s69
  s_mul_i32 s78, s78, 132
  s_add_u32 s72, s74, s78
  s_addc_u32 s73, s75, 0
  s_setpc_b64 s[72:73]
.Lch_dskip_%=:
  s_waitcnt vmcnt(0)
  s_barrier
  s_branch .Lch_dnext_%=
.Lch_ck_%=:
  ds_read_b64_tr_b16 a[0:1], v16 offset:0
  ds_read_b64_tr_b16 a[48:49], v17 offset:0
  ds_read_b64_tr_b16 a[2:3], v16 offset:1024
  ds_read_b64_tr_b16 a[50:51], v17 offset:1024
  ds_read_b64_tr_b16 a[4:5], v16 offset:2048
  ds_read_b64_tr_b16 a[52:53], v17 offset:2048
  ds_read_b64_tr_b16 a[6:7], v16 offset:3072
  ds_read_b64_tr_b16 a[54:55], v17 offset:3072
  ds_read_b64_tr_b16 a[8:9], v16 offset:4096
  ds_read_b64_tr_b16 a[56:57], v17 offset:4096
  ds_read_b64_tr_b16 a[10:11], v16 offset:5120
  ds_read_b64_tr_b16 a[58:59], v17 offset:5120
  ds_read_b64_tr_b16 a[12:13], v16 offset:6144
  ds_read_b64_tr_b16 a[60:61], v17 offset:6144
  ds_read_b64_tr_b16 a[14:15], v16 offset:7168
  ds_read_b64_tr_b16 a[62:63], v17 offset:7168
  s_branch .Lch_dsub_%=
  ds_read_b64_tr_b16 a[16:17], v16 offset:0
  ds_read_b64_tr_b16 a[64:65], v17 offset:0
  ds_read_b64_tr_b16 a[18:19], v16 offset:1024
  ds_read_b64_tr_b16 a[66:67], v17 offset:1024
  ds_read_b64_tr_b16 a[20:21], v16 offset:2048
  ds_read_b64_tr_b16 a[68:69], v17 offset:2048
  ds_read_b64_tr_b16 a[22:23], v16 offset:3072
  ds_read_b64_tr_b16 a[70:71], v17 offset:3072
  ds_read_b64_tr_b16 a[24:25], v16 offset:4096
  ds_read_b64_tr_b16 a[72:73], v17 offset:4096
  ds_read_b64_tr_b16 a[26:27], v16 offset:5120
  ds_read_b64_tr_b16 a[74:75], v17 offset:5120
  ds_read_b64_tr_b16 a[28:29], v16 offset:6144
  ds_read_b64_tr_b16 a[76:77], v17 offset:6144
  ds_read_b64_tr_b16 a[30:31], v16 offset:7168
  ds_read_b64_tr_b16 a[78:79], v17 offset:7168
  s_branch .Lch_dsub_%=
  ds_read_b64_tr_b16 a[32:33], v16 offset:0
  ds_read_b64_tr_b16 a[80:81], v17 offset:0
  ds_read_b64_tr_b16 a[34:35], v16 offset:1024
  ds_read_b64_tr_b16 a[82:83], v17 offset:1024
  ds_read_b64_tr_b16 a[36:37], v16 offset:2048
  ds_read_b64_tr_b16 a[84:85], v17 offset:2048
  ds_read_b64_tr_b16 a[38:39], v16 offset:3072
  ds_read_b64_tr_b16 a[86:87], v17 offset:3072
  ds_read_b64_tr_b16 a[40:41], v16 offset:4096
  ds_read_b64_tr_b16 a[88:89], v17 offset:4096
  ds_read_b64_tr_b16 a[42:43], v16 offset:5120
  ds_read_b64_tr_b16 a[90:91], v17 offset:5120
  ds_read_b64_tr_b16 a[44:45], v16 offset:6144
  ds_read_b64_tr_b16 a[92:93], v17 offset:6144
  ds_read_b64_tr_b16 a[46:47], v16 offset:7168
  ds_read_b64_tr_b16 a[94:95], v17 offset:7168
  s_branch .Lch_dsub_%=
  ds_read_b64_tr_b16 v[20:21], v16 offset:0
  ds_read_b64_tr_b16 v[36:37], v17 offset:0
  ds_read_b64_tr_b16 v[22:23], v16 offset:1024
  ds_read_b64_tr_b16 v[38:39], v17 offset:1024
  ds_read_b64_tr_b16 v[24:25], v16 offset:2048
  ds_read_b64_tr_b16 v[40:41], v17 offset:2048
  ds_read_b64_tr_b16 v[26:27], v16 offset:3072
  ds_read_b64_tr_b16 v[42:43], v17 offset:3072
  ds_read_b64_tr_b16 v[28:29], v16 offset:4096
  ds_read_b64_tr_b16 v[44:45], v17 offset:4096
  ds_read_b64_tr_b16 v[30:31], v16 offset:5120
  ds_read_b64_tr_b16 v[46:47], v17 offset:5120
  ds_read_b64_tr_b16 v[32:33], v16 offset:6144
  ds_read_b64_tr_b16 v[48:49], v17 offset:6144
  ds_read_b64_tr_b16 v[34:35], v16 offset:7168
  ds_read_b64_tr_b16 v[50:51], v17 offset:7168
  s_branch .Lch_dsub_%=
  ds_read_b64_tr_b16 a[96:97], v16 offset:0
  ds_read_b64_tr_b16 a[144:145], v17 offset:0
  ds_read_b64_tr_b16 a[98:99], v16 offset:1024
  ds_read_b64_tr_b16 a[146:147], v17 offset:1024
  ds_read_b64_tr_b16 a[100:101], v16 offset:2048
  ds_read_b64_tr_b16 a[148:149], v17 offset:2048
  ds_read_b64_tr_b16 a[102:103], v16 offset:3072
  ds_read_b64_tr_b16 a[150:151], v17 offset:3072
  ds_read_b64_tr_b16 a[104:105], v16 offset:4096
  ds_read_b64_tr_b16 a[152:153], v17 offset:4096
  ds_read_b64_tr_b16 a[106:107], v16 offset:5120
  ds_read_b64_tr_b16 a[154:155], v17 offset:5120
  ds_read_b64_tr_b16 a[108:109], v16 offset:6144
  ds_read_b64_tr_b16 a[156:157], v17 offset:6144
  ds_read_b64_tr_b16 a[110:111], v16 offset:7168
  ds_read_b64_tr_b16 a[158:159], v17 offset:7168
  s_branch .Lch_dsub_%=
  ds_read_b64_tr_b16 a[112:113], v16 offset:0
  ds_read_b64_tr_b16 a[160:161], v17 offset:0
  ds_read_b64_tr_b16 a[114:115], v16 offset:1024
  ds_read_b64_tr_b16 a[162:163], v17 offset:1024
  ds_read_b64_tr_b16 a[116:117], v16 offset:2048
  ds_read_b64_tr_b16 a[164:165], v17 offset:2048
  ds_read_b64_tr_b16 a[118:119], v16 offset:3072
  ds_read_b64_tr_b16 a[166:167], v17 offset:3072
  ds_read_b64_tr_b16 a[120:121], v16 offset:4096
  ds_read_b64_tr_b16 a[168:169], v17 offset:4096
  ds_read_b64_tr_b16 a[122:123], v16 offset:5120
  ds_read_b64_tr_b16 a[170:171], v17 offset:5120
  ds_read_b64_tr_b16 a[124:125], v16 offset:6144
  ds_read_b64_tr_b16 a[172:173], v17 offset:6144
  ds_read_b64_tr_b16 a[126:127], v16 offset:7168
  ds_read_b64_tr_b16 a[174:175], v17 offset:7168
  s_branch .Lch_dsub_%=
  ds_read_b64_tr_b16 a[128:129], v16 offset:0
  ds_read_b64_tr_b16 a[176:177], v17 offset:0
  ds_read_b64_tr_b16 a[130:131], v16 offset:1024
  ds_read_b64_tr_b16 a[178:179], v17 offset:1024
  ds_read_b64_tr_b16 a[132:133], v16 offset:2048
  ds_read_b64_tr_b16 a[180:181], v17 offset:2048
  ds_read_b64_tr_b16 a[134:135], v16 offset:3072
  ds_read_b64_tr_b16 a[182:183], v17 offset:3072
  ds_read_b64_tr_b16 a[136:137], v16 offset:4096
  ds_read_b64_tr_b16 a[184:185], v17 offset:4096
  ds_read_b64_tr_b16 a[138:139], v16 offset:5120
  ds_read_b64_tr_b16 a[186:187], v17 offset:5120
  ds_read_b64_tr_b16 a[140:141], v16 offset:6144
  ds_read_b64_tr_b16 a[188:189], v17 offset:6144
  ds_read_b64_tr_b16 a[142:143], v16 offset:7168
  ds_read_b64_tr_b16 a[190:191], v17 offset:7168
  s_branch .Lch_dsub_%=
  ds_read_b64_tr_b16 v[52:53], v16 offset:0
  ds_read_b64_tr_b16 v[68:69], v17 offset:0
  ds_read_b64_tr_b16 v[54:55], v16 offset:1024
  ds_read_b64_tr_b16 v[70:71], v17 offset:1024
  ds_read_b64_tr_b16 v[56:57], v16 offset:2048
  ds_read_b64_tr_b16 v[72:73], v17 offset:2048
  ds_read_b64_tr_b16 v[58:59], v16 offset:3072
  ds_read_b64_tr_b16 v[74:75], v17 offset:3072
  ds_read_b64_tr_b16 v[60:61], v16 offset:4096
  ds_read_b64_tr_b16 v[76:77], v17 offset:4096
  ds_read_b64_tr_b16 v[62:63], v16 offset:5120
  ds_read_b64_tr_b16 v[78:79], v17 offset:5120
  ds_read_b64_tr_b16 v[64:65], v16 offset:6144
  ds_read_b64_tr_b16 v[80:81], v17 offset:6144
  ds_read_b64_tr_b16 v[66:67], v16 offset:7168
  ds_read_b64_tr_b16 v[82:83], v17 offset:7168
  s_branch .Lch_dsub_%=
  ds_read_b64_tr_b16 a[192:193], v16 offset:0
  ds_read_b64_tr_b16 a[240:241], v17 offset:0
  ds_read_b64_tr_b16 a[194:195], v16 offset:1024
  ds_read_b64_tr_b16 a[242:243], v17 offset:1024
  ds_read_b64_tr_b16 a[196:197], v16 offset:2048
  ds_read_b64_tr_b16 a[244:245], v17 offset:2048
  ds_read_b64_tr_b16 a[198:199], v16 offset:3072
  ds_read_b64_tr_b16 a[246:247], v17 offset:3072
  ds_read_b64_tr_b16 a[200:201], v16 offset:4096
  ds_read_b64_tr_b16 a[248:249], v17 offset:4096
  ds_read_b64_tr_b16 a[202:203], v16 offset:5120
  ds_read_b64_tr_b16 a[250:251], v17 offset:5120
  ds_read_b64_tr_b16 a[204:205], v16 offset:6144
  ds_read_b64_tr_b16 a[252:253], v17 offset:6144
  ds_read_b64_tr_b16 a[206:207], v16 offset:7168
  ds_read_b64_tr_b16 a[254:255], v17 offset:7168
  s_branch .Lch_dsub_%=
  ds_read_b64_tr_b16 a[208:209], v16 offset:0
  ds_read_b64_tr_b16 v[128:129], v17 offset:0
  ds_read_b64_tr_b16 a[210:211], v16 offset:1024
  ds_read_b64_tr_b16 v[130:131], v17 offset:1024
  ds_read_b64_tr_b16 a[212:213], v16 offset:2048
  ds_read_b64_tr_b16 v[132:133], v17 offset:2048
  ds_read_b64_tr_b16 a[214:215], v16 offset:3072
  ds_read_b64_tr_b16 v[134:135], v17 offset:3072
  ds_read_b64_tr_b16 a[216:217], v16 offset:4096
  ds_read_b64_tr_b16 v[136:137], v17 offset:4096
  ds_read_b64_tr_b16 a[218:219], v16 offset:5120
  ds_read_b64_tr_b16 v[138:139], v17 offset:5120
  ds_read_b64_tr_b16 a[220:221], v16 offset:6144
  ds_read_b64_tr_b16 v[140:141], v17 offset:6144
  ds_read_b64_tr_b16 a[222:223], v16 offset:7168
  ds_read_b64_tr_b16 v[142:143], v17 offset:7168
  s_branch .Lch_dsub_%=
  ds_read_b64_tr_b16 a[224:225], v16 offset:0
  ds_read_b64_tr_b16 v[144:145], v17 offset:0
  ds_read_b64_tr_b16 a[226:227], v16 offset:1024
  ds_read_b64_tr_b16 v[146:147], v17 offset:1024
  ds_read_b64_tr_b16 a[228:229], v16 offset:2048
  ds_read_b64_tr_b16 v[148:149], v17 offset:2048
  ds_read_b64_tr_b16 a[230:231], v16 offset:3072
  ds_read_b64_tr_b16 v[150:151], v17 offset:3072
  ds_read_b64_tr_b16 a[232:233], v16 offset:4096
  ds_read_b64_tr_b16 v[152:153], v17 offset:4096
  ds_read_b64_tr_b16 a[234:235], v16 offset:5120
  ds_read_b64_tr_b16 v[154:155], v17 offset:5120
  ds_read_b64_tr_b16 a[236:237], v16 offset:6144
  ds_read_b64_tr_b16 v[156:157], v17 offset:6144
  ds_read_b64_tr_b16 a[238:239], v16 offset:7168
  ds_read_b64_tr_b16 v[158:159], v17 offset:7168
  s_branch .Lch_dsub_%=
  ds_read_b64_tr_b16 v[84:85], v16 offset:0
  ds_read_b64_tr_b16 v[100:101], v17 offset:0
  ds_read_b64_tr_b16 v[86:87], v16 offset:1024
  ds_read_b64_tr_b16 v[102:103], v17 offset:1024
  ds_read_b64_tr_b16 v[88:89], v16 offset:2048
  ds_read_b64_tr_b16 v[104:105], v17 offset:2048
  ds_read_b64_tr_b16 v[90:91], v16 offset:3072
  ds_read_b64_tr_b16 v[106:107], v17 offset:3072
  ds_read_b64_tr_b16 v[92:93], v16 offset:4096
  ds_read_b64_tr_b16 v[108:109], v17 offset:4096
  ds_read_b64_tr_b16 v[94:95], v16 offset:5120
  ds_read_b64_tr_b16 v[110:111], v17 offset:5120
  ds_read_b64_tr_b16 v[96:97], v16 offset:6144
  ds_read_b64_tr_b16 v[112:113], v17 offset:6144
  ds_read_b64_tr_b16 v[98:99], v16 offset:7168
  ds_read_b64_tr_b16 v[114:115], v17 offset:7168
  s_branch .Lch_dsub_%=
.Lch_dexit_%=:
  s_branch .Lch_go1_%=
.Lch_go0_%=:
  s_waitcnt vmcnt(0)
  s_and_b32 vcc_lo, s38, 15
  s_cbranch_scc1 .Lch_go2_%=
  s_add_u32 s15, s15, 16
  v_mov_b32 v18, s15
  v_mov_b32 v19, -1
  s_mov_b64 exec, 1
  buffer_store_dwordx2 v[18:19], off, s[20:23], s14 sc1
  s_mov_b64 exec, -1
.Lch_go2_%=:
  s_add_u32 s38, s38, 1
  s_cmp_gt_u32 s38, 0x40000
  s_cbranch_scc1 .Lch_end_%=
  s_sleep 2
  buffer_load_dword v18, off, s[20:23], s13 sc1
  s_waitcnt vmcnt(0)
.Lch_go1_%=:
  v_readfirstlane_b32 vcc_lo, v18
  s_nop 3
  s_cmp_eq_u32 vcc_lo, s12
  s_cbranch_scc1 .Lch_go0_%=
  s_mov_b32 s12, vcc_lo
  s_bitcmp0_b32 s12, 2
  s_cbranch_scc1 .Lch_go3_%=
  s_lshl_b32 s2, s24, 6
  v_mbcnt_lo_u32_b32 v0, -1, s2
  v_mbcnt_hi_u32_b32 v0, -1, v0
  s_mov_b64 s[0:1], s[16:17]
  s_mov_b32 s2, s18
  s_waitcnt vmcnt(0) lgkmcnt(0)
  s_barrier
  s_branch _ZN3rmt11main_kernelENS_6ParamsE
.Lch_go3_%=:
  s_lshl_b32 s39, s12, s19
  s_and_b32 s39, s39, 0x2000
  s_lshl_b32 s43, s12, 13
  s_and_b32 s43, s43, 0x2000
  s_xor_b32 s25, s25, s39
  s_xor_b32 s26, s26, s39
  s_xor_b32 s27, s27, s39
  s_xor_b32 s28, s28, s39
  s_xor_b32 s33, s33, s39
  s_xor_b32 s30, s30, s43
  s_xor_b32 s31, s31, s43
  s_sub_u32 s7, s31, s6
  s_sub_u32 s7, s7, 24576
  s_add_u32 s10, s27, 65536
  s_waitcnt vmcnt(27)
  ds_read_b64_tr_b16 v[160:161], v13 offset:0
  ds_read_b64_tr_b16 v[162:163], v13 offset:512
  ds_read_b64_tr_b16 v[192:193], v13 offset:32
  ds_read_b64_tr_b16 v[194:195], v13 offset:544
  ds_read_b64_tr_b16 v[224:225], v13 offset:128
  ds_read_b64_tr_b16 v[226:227], v13 offset:640
  ds_read_b64_tr_b16 v[240:241], v13 offset:160
  ds_read_b64_tr_b16 v[242:243], v13 offset:672
  ds_read_b64_tr_b16 v[164:165], v13 offset:2048
  ds_read_b64_tr_b16 v[166:167], v13 offset:2560
  ds_read_b64_tr_b16 v[196:197], v13 offset:2080
  ds_read_b64_tr_b16 v[198:199], v13 offset:2592
  ds_read_b64_tr_b16 v[228:229], v13 offset:2176
  ds_read_b64_tr_b16 v[230:231], v13 offset:2688
  ds_read_b64_tr_b16 v[244:245], v13 offset:2208
  ds_read_b64_tr_b16 v[246:247], v13 offset:2720
  ds_write_b128 v121, v[20:23] offset:0
  ds_write_b128 v121, v[24:27] offset:1024
  ds_write_b128 v121, v[28:31] offset:2048
  ds_write_b128 v121, v[32:35] offset:3072
  ds_write_b128 v121, v[36:39] offset:4096
  ds_write_b128 v121, v[40:43] offset:5120
  ds_write_b128 v121, v[44:47] offset:6144
  ds_write_b128 v121, v[48:51] offset:7168
  s_waitcnt vmcnt(18)
  ds_read_b64_tr_b16 v[168:169], v13 offset:8192
  ds_read_b64_tr_b16 v[170:171], v13 offset:8704
  ds_read_b64_tr_b16 v[200:201], v13 offset:8224
  ds_read_b64_tr_b16 v[202:203], v13 offset:8736
  ds_read_b64_tr_b16 v[232:233], v13 offset:8320
  ds_read_b64_tr_b16 v[234:235], v13 offset:8832
  ds_read_b64_tr_b16 v[248:249], v13 offset:8352
  ds_read_b64_tr_b16 v[250:251], v13 offset:8864
  ds_read_b64_tr_b16 v[172:173], v13 offset:10240
  ds_read_b64_tr_b16 v[174:175], v13 offset:10752
  ds_read_b64_tr_b16 v[204:205], v13 offset:10272
  ds_read_b64_tr_b16 v[206:207], v13 offset:10784
  ds_read_b64_tr_b16 v[236:237], v13 offset:10368
  ds_read_b64_tr_b16 v[238:239], v13 offset:10880
  ds_read_b64_tr_b16 v[252:253], v13 offset:10400
  ds_read_b64_tr_b16 v[254:255], v13 offset:10912
  ds_write_b128 v121, v[52:55] offset:8192
  ds_write_b128 v121, v[56:59] offset:9216
  ds_write_b128 v121, v[60:63] offset:10240
  ds_write_b128 v121, v[64:67] offset:11264
  ds_write_b128 v121, v[68:71] offset:12288
  ds_write_b128 v121, v[72:75] offset:13312
  ds_write_b128 v121, v[76:79] offset:14336
  ds_write_b128 v121, v[80:83] offset:15360
  s_waitcnt vmcnt(9)
  ds_read_b64_tr_b16 v[176:177], v13 offset:16384
  ds_read_b64_tr_b16 v[178:179], v13 offset:16896
  ds_read_b64_tr_b16 v[208:209], v13 offset:16416
  ds_read_b64_tr_b16 v[210:211], v13 offset:16928
  ds_read_b64_tr_b16 v[20:21], v13 offset:16512
  ds_read_b64_tr_b16 v[22:23], v13 offset:17024
  ds_read_b64_tr_b16 v[36:37], v13 offset:16544
  ds_read_b64_tr_b16 v[38:39], v13 offset:17056
  ds_read_b64_tr_b16 v[180:181], v13 offset:18432
  ds_read_b64_tr_b16 v[182:183], v13 offset:18944
  ds_read_b64_tr_b16 v[212:213], v13 offset:18464
  ds_read_b64_tr_b16 v[214:215], v13 offset:18976
  ds_read_b64_tr_b16 v[24:25], v13 offset:18560
  ds_read_b64_tr_b16 v[26:27], v13 offset:19072
  ds_read_b64_tr_b16 v[40:41], v13 offset:18592
  ds_read_b64_tr_b16 v[42:43], v13 offset:19104
  ds_write_b128 v121, v[84:87] offset:16384
  ds_write_b128 v121, v[88:91] offset:17408
  ds_write_b128 v121, v[92:95] offset:18432
  ds_write_b128 v121, v[96:99] offset:19456
  ds_write_b128 v121, v[100:103] offset:20480
  ds_write_b128 v121, v[104:107] offset:21504
  ds_write_b128 v121, v[108:111] offset:22528
  ds_write_b128 v121, v[112:115] offset:23552
  buffer_load_dword v19, off, s[20:23], s33 sc1
  s_waitcnt vmcnt(1)
  ds_read_b64_tr_b16 v[184:185], v13 offset:24576
  ds_read_b64_tr_b16 v[186:187], v13 offset:25088
  ds_read_b64_tr_b16 v[216:217], v13 offset:24608
  ds_read_b64_tr_b16 v[218:219], v13 offset:25120
  ds_read_b64_tr_b16 v[28:29], v13 offset:24704
  ds_read_b64_tr_b16 v[30:31], v13 offset:25216
  ds_read_b64_tr_b16 v[44:45], v13 offset:24736
  ds_read_b64_tr_b16 v[46:47], v13 offset:25248
  ds_read_b64_tr_b16 v[188:189], v13 offset:26624
  ds_read_b64_tr_b16 v[190:191], v13 offset:27136
  ds_read_b64_tr_b16 v[220:221], v13 offset:26656
  ds_read_b64_tr_b16 v[222:223], v13 offset:27168
  ds_read_b64_tr_b16 v[32:33], v13 offset:26752
  ds_read_b64_tr_b16 v[34:35], v13 offset:27264
  ds_read_b64_tr_b16 v[48:49], v13 offset:26784
  ds_read_b64_tr_b16 v[50:51], v13 offset:27296
  s_waitcnt lgkmcnt(15)
  ds_write_b128 v121, v[20:23] offset:24576
  ds_write_b128 v121, v[24:27] offset:25600
  s_waitcnt lgkmcnt(12)
  ds_write_b128 v121, v[28:31] offset:26624
  s_waitcnt lgkmcnt(5)
  ds_write_b128 v121, v[32:35] offset:27648
  ds_write_b128 v121, v[36:39] offset:28672
  ds_write_b128 v121, v[40:43] offset:29696
  ds_write_b128 v121, v[44:47] offset:30720
  s_waitcnt lgkmcnt(7)
  ds_write_b128 v121, v[48:51] offset:31744
  .if DBGT
  s_memrealtime s[98:99]
  s_waitcnt lgkmcnt(0)
  v_mov_b32 v10, s98
  s_mov_b64 exec, 1
  buffer_store_dword v10, off, s[20:23], s54 offset:2044
  s_mov_b64 exec, -1
  .endif
  s_add_u32 m0, s46, 8192
  s_sub_u32 s24, s25, s45
  s_sub_u32 s39, s26, s24
  s_add_u32 s24, s24, 1576960
  s_cmp_eq_u32 s39, 17408
  s_cselect_b32 s55, s24, 0xf0000000
  s_bitcmp1_b32 s39, 10
  s_cselect_b32 s79, 0, CH_E1
  s_memrealtime s[98:99]
  s_waitcnt lgkmcnt(0)
  s_mov_b32 s41, s98
  s_waitcnt vmcnt(0)
  v_readfirstlane_b32 s39, v19
  s_mov_b32 s43, 1
  s_nop 2
  s_cmp_lg_u32 s39, 0
  s_cbranch_scc1 .Lch_kvgo_%=
  s_branch .Lch_kvwait_%=
.Lch_abort_%=:
  v_mov_b32 v0, 1
  s_mov_b64 exec, 1
  buffer_store_dword v0, off, s[20:23], 0 offset:0 sc1
  s_mov_b64 exec, -1
  s_branch .Lch_end_%=
.Lch_mretry_%=:
  s_nop 7
  v_mov_b64 v[32:33], 0
  v_mov_b64 v[34:35], 0
  v_mov_b64 v[36:37], 0
  v_mov_b64 v[38:39], 0
  v_mov_b64 v[40:41], 0
  v_mov_b64 v[42:43], 0
  v_mov_b64 v[44:45], 0
  v_mov_b64 v[46:47], 0
  v_mov_b64 v[48:49], 0
  v_mov_b64 v[50:51], 0
  v_mov_b64 v[52:53], 0
  v_mov_b64 v[54:55], 0
  s_add_u32 s38, s38, 1
  s_cmp_gt_u32 s38, 0x40000
  s_cbranch_scc1 .Lch_abort_%=
  s_branch .Lch_mpoll_%=
.Lch_iretry_%=:
  s_add_u32 s38, s38, 1
  s_cmp_gt_u32 s38, 0x40000
  s_cbranch_scc1 .Lch_abort_%=
  s_nop 7
  s_memrealtime s[98:99]
  s_mov_b32 s78, 1
  buffer_load_dwordx4 v[0:3], v120, s[20:23], s28 offen offset:0 sc1
  buffer_load_dwordx4 v[4:7], v120, s[20:23], s28 offen offset:1024 sc1
  s_waitcnt vmcnt(0) lgkmcnt(0)
  s_branch .Lch_icheck_%=
.Lch_kvwait_%=:
  s_add_u32 s38, s38, 1
  s_cmp_gt_u32 s38, 0x40000
  s_cbranch_scc1 .Lch_abort_%=
  s_add_u32 s43, s42, 1
  s_mov_b64 exec, 1
  v_mov_b32 v0, 0
  buffer_atomic_or v0, v120, s[20:23], s33 offen sc0 sc1
  s_waitcnt vmcnt(0)
  v_cmp_gt_u32 vcc, s43, v0
  s_mov_b64 exec, -1
  s_cbranch_vccnz .Lch_kvwait_%=
.Lch_kvgo_%=:
  buffer_load_dwordx4 v120, s[20:23], s30 offen offset:0 sc1 lds
  buffer_load_dwordx4 v120, s[20:23], s30 offen offset:1024 sc1 lds
  buffer_load_dwordx4 v120, s[20:23], s30 offen offset:2048 sc1 lds
  buffer_load_dwordx4 v120, s[20:23], s30 offen offset:3072 sc1 lds
  s_cmp_lg_u32 s43, 1
  s_cbranch_scc1 .Lch_top_%=
  ds_read_b128 v[56:59], v121 offset:0
  ds_read_b128 v[60:63], v121 offset:1024
  ds_read_b128 v[64:67], v121 offset:4096
  ds_read_b128 v[68:71], v121 offset:5120
  ds_read_b128 v[72:75], v121 offset:2048
  ds_read_b128 v[76:79], v121 offset:3072
  ds_read_b128 v[80:83], v121 offset:6144
  ds_read_b128 v[84:87], v121 offset:7168
  ds_read_b128 v[88:91], v121 offset:8192
  ds_read_b128 v[92:95], v121 offset:9216
  ds_read_b128 v[96:99], v121 offset:12288
  ds_read_b128 v[100:103], v121 offset:13312
  ds_read_b128 v[104:107], v121 offset:10240
  ds_read_b128 v[108:111], v121 offset:11264
  ds_read_b128 v[112:115], v121 offset:14336
  ds_read_b128 v[116:119], v121 offset:15360
  v_mov_b64 v[32:33], 0
  v_mov_b64 v[34:35], 0
  v_mov_b64 v[36:37], 0
  v_mov_b64 v[38:39], 0
  v_mov_b64 v[40:41], 0
  v_mov_b64 v[42:43], 0
  v_mov_b64 v[44:45], 0
  v_mov_b64 v[46:47], 0
  v_mov_b64 v[48:49], 0
  v_mov_b64 v[50:51], 0
  v_mov_b64 v[52:53], 0
  v_mov_b64 v[54:55], 0
  v_or_b32 v126, s45, v120
  v_xor_b32 v8, 0x800, v126
  v_xor_b32 v16, 0x1000, v126
  v_xor_b32 v24, 0x1800, v126
  s_memrealtime s[98:99]
  s_cmp_eq_u32 s19, 12
  s_cselect_b32 s39, CH_L1D, 0
  s_waitcnt lgkmcnt(0)
  s_add_u32 s41, s98, s39
.Lch_top_%=:
  s_add_u32 s33, s33, 16
  s_and_b32 s39, s33, 48
  s_cmp_eq_u32 s39, 48
  s_cselect_b32 s39, 48, 0
  s_sub_u32 s33, s33, s39
  s_and_b32 s32, s33, 48
  s_mul_i32 s32, s32, 1023
  s_add_u32 s32, s32, s33
  s_add_u32 s32, s32, CH_FM
  s_add_u32 s30, s30, 1048576
  s_bitcmp0_b32 s30, 20
  s_cselect_b32 s43, 0x2000, 0
  s_xor_b32 s30, s30, s43
  s_add_u32 s43, s42, 2
.Lch_w2_%=:
  s_memrealtime s[98:99]
  s_waitcnt lgkmcnt(0)
  s_sub_u32 s99, s98, s41
  s_cmp_lt_i32 s99, 0
  s_cbranch_scc1 .Lch_w2_%=
.Lch_mpoll_%=:
  buffer_load_dwordx4 v[0:3], v120, s[20:23], s25 offen offset:0 sc1
  buffer_load_dwordx4 v[4:7], v120, s[20:23], s25 offen offset:1024 sc1
  buffer_load_dword v10, off, s[20:23], s33 offset:0 sc1
  s_waitcnt vmcnt(2) lgkmcnt(0)
  ds_write_b128 v126, v[0:3] offset:0
  v_smfmac_f32_16x16x64_bf16 v[32:35], v[0:3], a[0:7], v125
  v_smfmac_f32_16x16x64_bf16 v[36:39], v[0:3], a[48:55], v125
  s_waitcnt vmcnt(1)
  ds_write_b128 v126, v[4:7] offset:1024
  v_smfmac_f32_16x16x64_bf16 v[40:43], v[0:3], a[96:103], v125
  v_max3_u32 v9, v0, v1, v2
  v_smfmac_f32_16x16x64_bf16 v[44:47], v[0:3], a[144:151], v125
  v_max3_u32 v9, v9, v3, v4
  v_smfmac_f32_16x16x64_bf16 v[48:51], v[0:3], a[192:199], v125
  v_max3_u32 v9, v9, v5, v6
  v_smfmac_f32_16x16x64_bf16 v[52:55], v[0:3], a[240:247], v125
  v_max_u32 v9, v9, v7
  v_cmp_eq_u32 vcc, -1, v9
  v_smfmac_f32_16x16x64_bf16 v[32:35], v[4:7], a[8:15], v125
  s_waitcnt vmcnt(0)
  v_readfirstlane_b32 s52, v10
  v_smfmac_f32_16x16x64_bf16 v[36:39], v[4:7], a[56:63], v125
  s_cbranch_vccnz .Lch_mretry_%=
  s_waitcnt lgkmcnt(0)
  s_barrier
  ds_read_b128 v[28:31], v24 offset:1024
  ds_read_b128 v[24:27], v24 offset:0
  v_smfmac_f32_16x16x64_bf16 v[40:43], v[4:7], a[104:111], v125
  ds_read_b128 v[12:15], v8 offset:1024
  ds_read_b128 v[8:11], v8 offset:0
  v_smfmac_f32_16x16x64_bf16 v[44:47], v[4:7], a[152:159], v125
  ds_read_b128 v[20:23], v16 offset:1024
  ds_read_b128 v[16:19], v16 offset:0
  v_smfmac_f32_16x16x64_bf16 v[48:51], v[4:7], a[200:207], v125
  s_memrealtime s[8:9]
  ds_read_b64 v[126:127], v120 offset:31752
  v_smfmac_f32_16x16x64_bf16 v[52:55], v[4:7], a[248:255], v125
  s_cmp_ge_u32 s52, s43
  s_cselect_b32 s52, s30, 0xf0000000
  v_or_b32 v3, s46, v120
  s_waitcnt lgkmcnt(6)
  v_smfmac_f32_16x16x64_bf16 v[32:35], v[28:31], v[72:79], v125
  ds_read_b64 v[4:5], v3 offset:11272
  s_cselect_b32 s49, 1, 0
  s_cselect_b32 s12, 0xf0000000, s33
  v_smfmac_f32_16x16x64_bf16 v[36:39], v[28:31], v[80:87], v125
  ds_read_b64 v[6:7], v3 offset:11264
  s_waitcnt lgkmcnt(7)
  v_smfmac_f32_16x16x64_bf16 v[32:35], v[24:27], v[56:63], v125
  v_smfmac_f32_16x16x64_bf16 v[36:39], v[24:27], v[64:71], v125
  s_waitcnt lgkmcnt(6)
  v_smfmac_f32_16x16x64_bf16 v[32:35], v[12:15], a[24:31], v125
  s_waitcnt lgkmcnt(5)
  v_smfmac_f32_16x16x64_bf16 v[32:35], v[8:11], a[16:23], v125
  s_waitcnt lgkmcnt(0)
  v_smfmac_f32_16x16x64_bf16 v[32:35], v[20:23], a[40:47], v125
  s_add_u32 s39, s8, s11
  v_smfmac_f32_16x16x64_bf16 v[32:35], v[16:19], a[32:39], v125
  ds_read_b128 v[72:75], v120 offset:9216
  v_mov_b32 v0, s39
  v_smfmac_f32_16x16x64_bf16 v[36:39], v[12:15], a[72:79], v125
  buffer_load_dwordx4 v120, s[20:23], s52 offen offset:3072 sc1 lds
  v_smfmac_f32_16x16x64_bf16 v[36:39], v[8:11], a[64:71], v125
  s_mov_b64 exec, 1
  buffer_store_dword v0, off, s[20:23], s55 offset:0 sc1
  s_mov_b64 exec, -1
  v_smfmac_f32_16x16x64_bf16 v[36:39], v[20:23], a[88:95], v125
  ds_read_b128 v[0:3], v3 offset:10240
  v_add_f32 v32, v32, v33
  v_smfmac_f32_16x16x64_bf16 v[36:39], v[16:19], a[80:87], v125
  ds_read_b128 v[76:79], v120 offset:13312
  v_add_f32 v34, v34, v35
  v_smfmac_f32_16x16x64_bf16 v[40:43], v[24:27], v[88:95], v125
  ds_read_b128 v[80:83], v120 offset:17408
  v_cvt_pk_bf16_f32 v33, v32, v34
  v_smfmac_f32_16x16x64_bf16 v[44:47], v[24:27], v[96:103], v125
  ds_read_b128 v[84:87], v120 offset:21504
  v_add_u32 v126, s47, v126
  v_smfmac_f32_16x16x64_bf16 v[40:43], v[28:31], v[104:111], v125
  ds_read_b128 v[56:59], v120 offset:8192
  v_add_f32 v36, v36, v37
  v_smfmac_f32_16x16x64_bf16 v[44:47], v[28:31], v[112:119], v125
  v_add_f32 v38, v38, v39
  v_cvt_pk_bf16_f32 v37, v36, v38
  v_smfmac_f32_16x16x64_bf16 v[40:43], v[8:11], a[112:119], v125
  ds_write2st64_b32 v126, v33, v37 offset0:0 offset1:1
  v_smfmac_f32_16x16x64_bf16 v[40:43], v[12:15], a[120:127], v125
  ds_read_b128 v[60:63], v120 offset:12288
  v_add_u32 v127, s6, v127
  v_smfmac_f32_16x16x64_bf16 v[40:43], v[16:19], a[128:135], v125
  ds_read_b128 v[64:67], v120 offset:16384
  v_smfmac_f32_16x16x64_bf16 v[40:43], v[20:23], a[136:143], v125
  ds_read_b128 v[68:71], v120 offset:20480
  v_smfmac_f32_16x16x64_bf16 v[44:47], v[8:11], a[160:167], v125
  s_waitcnt lgkmcnt(8)
  buffer_load_dwordx4 v120, s[20:23], s52 offen offset:2048 sc1 lds
  v_smfmac_f32_16x16x64_bf16 v[44:47], v[12:15], a[168:175], v125
  ds_read_b128 v[104:107], v121 offset:16384
  v_smfmac_f32_16x16x64_bf16 v[44:47], v[16:19], a[176:183], v125
  ds_read_b128 v[108:111], v121 offset:17408
  s_waitcnt lgkmcnt(2)
  s_barrier
  v_smfmac_f32_16x16x64_bf16 v[44:47], v[20:23], a[184:191], v125
  ds_read_b64_tr_b16 v[88:89], v122 offset:0
  ds_read_b64_tr_b16 v[90:91], v122 offset:256
  v_smfmac_f32_16x16x64_bf16 v[48:51], v[8:11], a[208:215], v125
  ds_read_b64_tr_b16 v[92:93], v122 offset:512
  ds_read_b64_tr_b16 v[94:95], v122 offset:768
  v_smfmac_f32_16x16x64_bf16 v[48:51], v[12:15], a[216:223], v125
  ds_read_b64_tr_b16 v[96:97], v122 offset:1024
  ds_read_b64_tr_b16 v[98:99], v122 offset:1280
  v_smfmac_f32_16x16x64_bf16 v[48:51], v[16:19], a[224:231], v125
  ds_read_b64_tr_b16 v[100:101], v122 offset:1536
  ds_read_b64_tr_b16 v[102:103], v122 offset:1792
  v_smfmac_f32_16x16x64_bf16 v[48:51], v[20:23], a[232:239], v125
  ds_read_b128 v[112:115], v121 offset:18432
  v_add_f32 v44, v44, v45
  s_waitcnt lgkmcnt(7)
  v_mfma_f32_16x16x32_bf16 v[32:35], v[56:59], v[88:91], 0
  s_waitcnt lgkmcnt(5)
  v_add_f32 v46, v46, v47
  v_add_f32 v40, v40, v41
  v_mfma_f32_16x16x32_bf16 v[32:35], v[60:63], v[92:95], v[32:35]
  s_waitcnt lgkmcnt(3)
  v_add_f32 v42, v42, v43
  v_cvt_pk_bf16_f32 v45, v44, v46
  v_mfma_f32_16x16x32_bf16 v[32:35], v[64:67], v[96:99], v[32:35]
  s_waitcnt lgkmcnt(1)
  v_cvt_pk_bf16_f32 v41, v40, v42
  v_mfma_f32_16x16x32_bf16 v[32:35], v[68:71], v[100:103], v[32:35]
  ds_write2st64_b32 v127, v41, v45 offset0:0 offset1:2
  v_mfma_f32_16x16x32_bf16 v[36:39], v[72:75], v[88:91], 0
  ds_read_b128 v[116:119], v121 offset:19456
  v_mfma_f32_16x16x32_bf16 v[36:39], v[76:79], v[92:95], v[36:39]
  buffer_load_dwordx4 v120, s[20:23], s52 offen offset:0 sc1 lds
  v_mfma_f32_16x16x32_bf16 v[36:39], v[80:83], v[96:99], v[36:39]
  buffer_load_dwordx4 v120, s[20:23], s52 offen offset:1024 sc1 lds
  v_mfma_f32_16x16x32_bf16 v[36:39], v[84:87], v[100:103], v[36:39]
  s_waitcnt lgkmcnt(1)
  s_barrier
  v_smfmac_f32_16x16x64_bf16 v[48:51], v[24:27], v[104:111], v125
  ds_read_b64_tr_b16 v[56:57], v123 offset:0
  ds_read_b64_tr_b16 v[58:59], v123 offset:512
  s_waitcnt lgkmcnt(2)
  v_smfmac_f32_16x16x64_bf16 v[48:51], v[28:31], v[112:119], v125
  ds_read_b64_tr_b16 v[60:61], v123 offset:1024
  ds_read_b64_tr_b16 v[62:63], v123 offset:1536
  v_smfmac_f32_16x16x64_bf16 v[52:55], v[8:11], v[128:135], v125
  ds_read_b64_tr_b16 v[64:65], v123 offset:2048
  ds_read_b64_tr_b16 v[66:67], v123 offset:2560
  v_smfmac_f32_16x16x64_bf16 v[52:55], v[12:15], v[136:143], v125
  ds_read_b64_tr_b16 v[68:69], v123 offset:3072
  ds_read_b64_tr_b16 v[70:71], v123 offset:3584
  s_waitcnt lgkmcnt(6)
  v_mfma_f32_16x16x32_bf16 v[40:43], v[56:59], v[88:91], 0
  ds_read_b128 v[72:75], v121 offset:20480
  v_cndmask_b32_e64 v32, v32, v36, s[34:35]
  s_waitcnt lgkmcnt(5)
  v_mfma_f32_16x16x32_bf16 v[40:43], v[60:63], v[92:95], v[40:43]
  ds_read_b128 v[76:79], v121 offset:21504
  v_cndmask_b32_e64 v33, v33, v37, s[34:35]
  s_waitcnt lgkmcnt(4)
  v_mfma_f32_16x16x32_bf16 v[40:43], v[64:67], v[96:99], v[40:43]
  ds_read_b128 v[80:83], v121 offset:22528
  v_cndmask_b32_e64 v34, v34, v38, s[34:35]
  s_waitcnt lgkmcnt(3)
  v_mfma_f32_16x16x32_bf16 v[40:43], v[68:71], v[100:103], v[40:43]
  ds_read_b128 v[84:87], v121 offset:23552
  v_cndmask_b32_e64 v35, v35, v39, s[34:35]
  v_smfmac_f32_16x16x64_bf16 v[52:55], v[16:19], v[144:151], v125
  ds_read2_b32 v[46:47], v127 offset0:1 offset1:129
  v_max_f32 v44, v32, v33
  v_max3_f32 v44, v44, v34, v35
  v_add_f32 v48, v48, v49
  v_add_f32 v50, v50, v51
  v_smfmac_f32_16x16x64_bf16 v[52:55], v[20:23], v[152:159], v125
  s_waitcnt lgkmcnt(3)
  v_max_f32_dpp v44, v44, v44 row_ror:8 row_mask:0xf bank_mask:0xf
  v_max3_f32 v44, v44, v40, v41
  v_mov_b32 v45, v44
  v_smfmac_f32_16x16x64_bf16 v[52:55], v[24:27], v[72:79], v125
  s_waitcnt lgkmcnt(1)
  v_permlane16_swap_b32 v44, v45
  v_max_f32 v44, v44, v45
  v_mov_b32 v45, v44
  v_smfmac_f32_16x16x64_bf16 v[52:55], v[28:31], v[80:87], v125
  ds_read_b128 v[56:59], v121 offset:24576
  v_permlane32_swap_b32 v44, v45
  v_max_f32 v44, v44, v45
  v_mul_f32 v44, s44, v44
  v_fma_f32 v40, v40, s44, -v44
  v_fma_f32 v41, v41, s44, -v44
  v_exp_f32 v40, v40
  v_exp_f32 v41, v41
  v_mov_b64 v[108:109], 0
  v_cvt_pk_bf16_f32 v45, v40, v41
  v_add_f32 v52, v52, v53
  v_add_f32 v54, v54, v55
  v_cndmask_b32_e64 v90, v45, 0, s[34:35]
  v_cndmask_b32_e64 v91, 0, v45, s[34:35]
  v_cvt_pk_bf16_f32 v110, v48, v50
  v_cvt_pk_bf16_f32 v111, v52, v54
  v_fma_f32 v32, v32, s44, -v44
  v_fma_f32 v33, v33, s44, -v44
  v_mfma_f32_16x16x32_bf16 v[116:119], v[108:111], v[88:91], 0
  v_fma_f32 v34, v34, s44, -v44
  v_fma_f32 v35, v35, s44, -v44
  v_exp_f32 v32, v32
  v_exp_f32 v33, v33
  v_exp_f32 v34, v34
  v_cvt_pk_bf16_f32 v92, v32, v33
  v_exp_f32 v35, v35
  v_mov_b64 v[104:105], s[4:5]
  v_cvt_pk_bf16_f32 v93, v34, v35
  v_mov_b64 v[106:107], s[50:51]
  v_mov_b32_dpp v94, v92 row_ror:8 row_mask:0xf bank_mask:0xf
  v_mov_b32_dpp v95, v93 row_ror:8 row_mask:0xf bank_mask:0xf
  v_mfma_f32_16x16x32_bf16 v[112:115], v[104:107], v[90:93], 0
  v_add_u32 v126, s6, v123
  v_mov_b64 v[8:9], 0
  v_mfma_f32_16x16x32_bf16 v[96:99], v[0:3], v[92:95], v[116:119]
  ds_read_b64_tr_b16 v[36:37], v126 offset:0
  ds_read_b64_tr_b16 v[38:39], v126 offset:512
  v_mfma_f32_16x16x32_bf16 v[100:103], v[4:7], v[92:95], v[116:119]
  v_mov_b64 v[10:11], 0
  v_mov_b64 v[12:13], 0
  v_add_f32_dpp v45, v112, v112 row_ror:8 row_mask:0xf bank_mask:0xf
  v_rcp_f32 v45, v45
  v_mov_b64 v[14:15], 0
  v_mul_f32 v96, v96, v45
  v_mul_f32 v97, v97, v45
  v_mul_f32 v98, v98, v45
  v_mul_f32 v99, v99, v45
  v_mul_f32_dpp v96, v100, v45 quad_perm:[0,1,2,3] row_mask:0xf bank_mask:0xc
  v_mul_f32_dpp v97, v101, v45 quad_perm:[0,1,2,3] row_mask:0xf bank_mask:0xc
  v_mul_f32_dpp v98, v102, v45 quad_perm:[0,1,2,3] row_mask:0xf bank_mask:0xc
  v_mul_f32_dpp v99, v103, v45 quad_perm:[0,1,2,3] row_mask:0xf bank_mask:0xc
  v_cvt_pk_bf16_f32 v100, v96, v97
  v_cvt_pk_bf16_f32 v101, v98, v99
  ds_write_b64 v124, v[100:101] offset:24576
  s_waitcnt lgkmcnt(4)
  ds_read_b128 v[48:51], v47 offset:0
  buffer_store_dwordx2 v[110:111], v127, s[20:23], s7 offen offset:0 sc1
  v_xor_b32 v24, 0x200, v47
  v_xor_b32 v28, 0x400, v47
  v_xor_b32 v52, 0x600, v47
  v_mov_b64 v[16:17], 0
  v_mov_b64 v[18:19], 0
  s_waitcnt lgkmcnt(1)
  s_barrier
  ds_read_b128 v[24:27], v24 offset:0
  s_waitcnt lgkmcnt(1)
  v_smfmac_f32_16x16x64_bf16 v[8:11], v[48:51], v[160:167], v125
  ds_read_b128 v[28:31], v28 offset:0
  v_mov_b64 v[20:21], 0
  v_smfmac_f32_16x16x64_bf16 v[12:15], v[48:51], v[192:199], v125
  ds_read_b128 v[52:55], v52 offset:0
  v_mov_b64 v[22:23], 0
  v_smfmac_f32_16x16x64_bf16 v[16:19], v[48:51], v[224:231], v125
  buffer_store_dwordx4 v[36:39], v46, s[20:23], s31 offen offset:0 sc1
  v_smfmac_f32_16x16x64_bf16 v[20:23], v[48:51], v[240:247], v125
  ds_read_b128 v[60:63], v121 offset:25600
  s_waitcnt lgkmcnt(3)
  v_smfmac_f32_16x16x64_bf16 v[8:11], v[24:27], v[168:175], v125
  ds_read_b128 v[64:67], v121 offset:26624
  v_smfmac_f32_16x16x64_bf16 v[12:15], v[24:27], v[200:207], v125
  ds_read_b128 v[68:71], v121 offset:27648
  s_waitcnt lgkmcnt(4)
  v_smfmac_f32_16x16x64_bf16 v[8:11], v[28:31], v[176:183], v125
  ds_read_b64 v[126:127], v120 offset:31744
  s_waitcnt lgkmcnt(4)
  v_smfmac_f32_16x16x64_bf16 v[8:11], v[52:55], v[184:191], v125
  ds_read_b128 v[72:75], v121 offset:28672
  v_smfmac_f32_16x16x64_bf16 v[12:15], v[28:31], v[208:215], v125
  ds_read_b128 v[76:79], v121 offset:29696
  v_smfmac_f32_16x16x64_bf16 v[12:15], v[52:55], v[216:223], v125
  ds_read_b128 v[80:83], v121 offset:30720
  v_smfmac_f32_16x16x64_bf16 v[16:19], v[24:27], v[232:239], v125
  ds_read_b128 v[84:87], v121 offset:31744
  buffer_load_dword v45, off, s[20:23], s12 offset:0 sc1
  v_cvt_pk_bf16_f32 v0, v8, v9
  s_waitcnt lgkmcnt(7)
  v_smfmac_f32_16x16x64_bf16 v[16:19], v[28:31], v[56:63], v125
  buffer_load_dword v44, off, s[20:23], s24 offset:0 sc1
  v_cvt_pk_bf16_f32 v2, v10, v11
  v_and_or_b32 v0, v0, -2, s29
  v_and_or_b32 v2, v2, -2, s29
  s_waitcnt lgkmcnt(5)
  v_smfmac_f32_16x16x64_bf16 v[16:19], v[52:55], v[64:71], v125
  v_cvt_pk_bf16_f32 v1, v12, v13
  v_cvt_pk_bf16_f32 v3, v14, v15
  v_smfmac_f32_16x16x64_bf16 v[20:23], v[24:27], v[248:255], v125
  s_waitcnt lgkmcnt(4)
  buffer_store_dwordx4 v[0:3], v126, s[20:23], s27 offen offset:0 sc1
  s_waitcnt lgkmcnt(2)
  v_smfmac_f32_16x16x64_bf16 v[20:23], v[28:31], v[72:79], v125
  s_cmp_eq_u32 s42, 63
  s_waitcnt lgkmcnt(0)
  v_smfmac_f32_16x16x64_bf16 v[20:23], v[52:55], v[80:87], v125
  v_cvt_pk_bf16_f32 v4, v16, v17
  v_cvt_pk_bf16_f32 v6, v18, v19
  v_and_or_b32 v4, v4, -2, s29
  v_and_or_b32 v6, v6, -2, s29
  s_nop 3
  v_cvt_pk_bf16_f32 v5, v20, v21
  v_cvt_pk_bf16_f32 v7, v22, v23
  buffer_store_dwordx4 v[4:7], v126, s[20:23], s10 offen offset:0 sc1
  s_memrealtime s[98:99]
  s_cbranch_scc1 .Lch_last_%=
  v_mov_b32 v28, v127
  ds_read_b128 v[12:15], v120 offset:30720
  ds_read_b128 v[56:59], v121 offset:0
  ds_read_b128 v[60:63], v121 offset:1024
  ds_read_b128 v[64:67], v121 offset:4096
  ds_read_b128 v[68:71], v121 offset:5120
  ds_read_b128 v[72:75], v121 offset:2048
  ds_read_b128 v[76:79], v121 offset:3072
  ds_read_b128 v[80:83], v121 offset:6144
  ds_read_b128 v[84:87], v121 offset:7168
  s_waitcnt lgkmcnt(0)
  s_add_u32 s42, s42, 1
  v_mov_b32 v31, s42
  s_waitcnt vmcnt(2)
  s_mov_b64 exec, 1
  buffer_store_dword v31, off, s[20:23], s32 offset:0 sc1
  s_mov_b64 exec, -1
  v_readfirstlane_b32 s40, v44
  v_readfirstlane_b32 s12, v45
  s_sub_u32 s53, s40, s41
  s_sub_u32 s53, s53, s11
  s_cmp_gt_i32 s53, CH_B1MAX
  s_cselect_b32 s53, CH_DLATE, 0
  s_sub_u32 s82, s98, s8
  s_add_u32 s82, s82, CH_D1L
  s_sub_u32 s11, s11, CH_DEC
  s_add_u32 s43, s82, CH_CAP
  s_min_u32 s11, s11, s43
  s_max_u32 s11, s11, s82
  s_min_u32 s11, s11, CH_P0
  s_sub_u32 s43, s40, s98
  s_add_u32 s53, s53, s43
  s_max_i32 s53, s53, CH_D1MIN
  s_add_u32 s40, s98, s53
  s_add_u32 s41, s40, CH_P2
  s_sub_u32 s40, s40, s79
  s_add_u32 s98, s98, CH_D1F
  s_add_u32 s43, s43, CH_TOL
  s_cmp_gt_u32 s43, 2*CH_TOL
  s_cselect_b32 s40, s98, s40
  s_cselect_b32 s78, 1, 0
.Lch_w1_%=:
  s_memrealtime s[98:99]
  s_waitcnt lgkmcnt(0)
  s_sub_u32 s99, s98, s40
  s_cmp_lt_i32 s99, 0
  s_cbranch_scc1 .Lch_w1_%=
  buffer_load_dwordx4 v[0:3], v120, s[20:23], s28 offen offset:0 sc1
  buffer_load_dwordx4 v[4:7], v120, s[20:23], s28 offen offset:1024 sc1
  s_waitcnt vmcnt(0) lgkmcnt(0)
.Lch_icheck_%=:
  v_mfma_f32_16x16x32_bf16 v[20:23], v[12:15], v[0:3], 0
  v_bitop3_b32 v29, v0, v2, s29 bitop3:0x7e
  v_bitop3_b32 v30, v4, v6, s29 bitop3:0x7e
  v_mfma_f32_16x16x32_bf16 v[20:23], v[12:15], v[4:7], v[20:23]
  v_bitop3_b32 v29, v29, v30, 1 bitop3:0xa8
  v_cmp_ne_u32 vcc, 0, v29
  s_cbranch_vccnz .Lch_iretry_%=
  s_nop 4
  v_add_f32_dpp v20, v20, v20 row_ror:4 row_mask:0xf bank_mask:0xf
  v_add_f32_dpp v21, v21, v21 row_ror:4 row_mask:0xf bank_mask:0xf
  s_nop 0
  v_add_f32_dpp v20, v20, v20 row_ror:8 row_mask:0xf bank_mask:0xf
  v_add_f32_dpp v21, v21, v21 row_ror:8 row_mask:0xf bank_mask:0xf
  v_cvt_pk_bf16_f32 v20, v20, v21
  v_min_u32 v20, -2, v20
  buffer_store_dword v20, v28, s[20:23], s26 offen offset:0 sc1
  s_add_u32 s26, s26, 16384
  s_add_u32 s25, s25, 16384
  s_sub_u32 s7, s7, s31
  s_add_u32 s31, s31, 262144
  s_and_b32 s43, s42, 7
  s_cselect_b32 s43, 0, 0x2000
  s_xor_b32 s31, s31, s43
  s_add_u32 s7, s7, s31
  s_memrealtime s[80:81]
  ds_read_b128 v[88:91], v121 offset:8192
  ds_read_b128 v[92:95], v121 offset:9216
  ds_read_b128 v[96:99], v121 offset:12288
  ds_read_b128 v[100:103], v121 offset:13312
  ds_read_b128 v[104:107], v121 offset:10240
  ds_read_b128 v[108:111], v121 offset:11264
  ds_read_b128 v[112:115], v121 offset:14336
  ds_read_b128 v[116:119], v121 offset:15360
  v_mov_b64 v[32:33], 0
  v_mov_b64 v[34:35], 0
  v_mov_b64 v[36:37], 0
  v_mov_b64 v[38:39], 0
  v_mov_b64 v[40:41], 0
  v_mov_b64 v[42:43], 0
  v_mov_b64 v[44:45], 0
  v_mov_b64 v[46:47], 0
  v_mov_b64 v[48:49], 0
  v_mov_b64 v[50:51], 0
  v_mov_b64 v[52:53], 0
  v_mov_b64 v[54:55], 0
  v_or_b32 v126, s45, v120
  v_xor_b32 v8, 0x800, v126
  v_xor_b32 v16, 0x1000, v126
  v_xor_b32 v24, 0x1800, v126
  s_and_b32 s29, s42, 1
  s_waitcnt lgkmcnt(0)
  s_cmp_lg_u32 s78, 0
  s_cselect_b32 s43, CH_D2F, 8
  s_sub_u32 s41, s41, s80
  s_cselect_b32 s41, 0, s41
  s_min_u32 s41, s41, 64
  s_max_u32 s41, s41, s43
  s_add_u32 s41, s41, s80
  s_cmp_lg_u32 s49, 0
  s_cbranch_scc1 .Lch_top_%=
  s_mov_b32 s43, 0
  s_cmp_gt_u32 s12, s42
  s_cbranch_scc1 .Lch_kvgo_%=
  s_branch .Lch_kvwait_%=
.Lch_last_%=:
  v_mov_b32 v30, 64
  s_waitcnt vmcnt(4)
  s_mov_b64 exec, 1
  buffer_store_dword v30, off, s[20:23], s32 offset:0 sc1
  s_mov_b64 exec, -1
.Lch_end_%=:
  s_waitcnt vmcnt(0) lgkmcnt(0)
  s_endpgm
)ASM"
    : "+{v[0:3]}"(asel), "+{v[4:7]}"(cst), "+{s25}"(a25), "+{s26}"(a26), "+{s30}"(a30), "+{s31}"(a31), "+{s24}"(a24), "+{s40}"(a40), "+{s41}"(a41), "+{s33}"(a33)
    : "{s[20:23]}"(srd), "{s27}"(s_inw), "{s28}"(s_inr), "{s54}"(s_dbg),
      "{s[56:59]}"(make_srd(p.wq, WBYTES)), "{s[60:61]}"(p.wq), "{s[62:63]}"(p.wk), "{s[64:65]}"(p.wv), "{s[66:67]}"(p.wo), "{s68}"(L * CHX), "{s[12:19]}"(go), "{v[10:13]}"(wl),
      "{v120}"(lane * 16), "{v8}"((c & 2) ? OOB : (u32)(g * 128 + ((c >> 2) * 2 + (c & 1)) * 16)), "{v9}"(o_rd), "{v122}"(q_rd), "{v123}"(k_rd), "{v124}"(o_wr), "{v125}"(idx)
    : ASM_TOP);
  __builtin_amdgcn_endpgm();
}

#define RF(x) __builtin_amdgcn_readfirstlane((u32)(x))
#define SLP 4
#define WDLY 0
#define QA_LEAD 14
#define QROT 1
#define QOFF 2
DEVI int qrot(int r) { return r + QOFF < 3 ? r + QOFF : r + QOFF - 3; }
DEVI const bf16* wrot(const Params& p, int i) { return i == 0 || i == 3 ? p.wk : i == 2 ? p.wq : p.wv; }
DEVI void qkva_role(const Params& p, const int w, const int h, const int r, const u32x8 go) {
  const u32 z0 = OFF_ZONE, z1 = OFF_ZONE + ZONE_BYTES, fb = 0;
  const int lane = lane_id();
  const int c = lane & 15, g = lane >> 4, mu = w >> 1, eh = w & 1;
  const u32x4 srd = make_srd(p.ws, WS_USED);
  const int rr = qrot(r), r1 = (r + 30 - QA_LEAD) % 3;
  const u32 n0 = (66 - r) / 3, n1 = (66 - r1) / 3, nl = (r1 + QA_LEAD - r) / 3;
  const u32 fkv = ZONE_FKV + h * 128 + w * 4;
  u64 ord = 0x5555555555555555ull << nl | ~0ull << (2 * n0 - nl);
  u32 n = n0 + n1;
  u32 o0 = RF(ord), o1 = RF(ord >> 32); n = RF(n);
  const u32 ninf = 0xff800000u;
  u32x4 m0, m1;
#pragma unroll
  for (int i = 0; i < 4; ++i) { const u32 cm = (4 * g + i) <= c ? 0u : ninf; m0[i] = mu ? 0u : cm; m1[i] = mu ? cm : ninf; }
  u32x4 xb;
#pragma unroll
  for (int i = 0; i < 4; ++i) xb[i] = 32 * KB + c * KB + ((4 * i + g) ^ c) * 16;
  const u32 pa = lane >= 40 ? OOB : lane < 32 ? fb + sp(F_H) + (r1 * 8 + (lane >> 2)) * 256 + (lane & 3) * 4 : (lane < 36 ? z0 + (r + 1) % 3 * 16 * KB : z1 + (r1 + 1) % 3 * 16 * KB) + ZONE_FKV + CH_FM + h * 128 + (lane & 3) * 4;
  asm volatile(DBGT ".set SLP, " STR(SLP) "\n" R"ASM(
  s_lshl_b32 s68, s24, 10
  s_mov_b32 s1, s87
  v_mov_b32 v84, 0
  v_mov_b64 v[68:69], 0
  v_mov_b64 v[70:71], 0
  v_mov_b64 v[72:73], 0
  v_mov_b64 v[74:75], 0
  v_mov_b64 v[76:77], 0
  s_add_u32 s36, s29, 0
  s_mov_b32 s38, s36
  s_mov_b32 s40, s36
  s_mov_b32 s42, -1
  s_add_u32 s37, s3, 64
  s_mov_b32 s39, s37
  s_mov_b32 s41, s37
  s_mov_b32 s43, -1
  s_mov_b32 s49, -1
  s_mov_b32 s55, 0
  s_mov_b32 s53, 0xf0000000
  s_mov_b32 s54, 0
  s_mov_b32 s58, 0x3e0293ee
  s_lshr_b32 s59, s24, 1
  s_lshl_b32 s59, s59, 12
  s_and_b32 s60, s24, 1
  s_lshl_b32 s60, s60, 11
  s_lshl_b32 s51, s24, 13
  s_xor_b32 s51, s51, 32768
  .if DBGT
  s_memrealtime s[70:71]
  s_waitcnt lgkmcnt(0)
  v_mov_b32 v10, s70
  s_mov_b64 exec, 1
  buffer_store_dword v10, off, s[20:23], s69 offset:56
  s_mov_b64 exec, -1
  .endif
  s_bitcmp1_b32 s26, 0
  s_cselect_b32 s45, s37, s36
  s_cselect_b32 s47, 0, 1
  s_cselect_b32 s56, 0, 3
  s_cselect_b32 s57, 3, 0
  s_add_u32 s36, s36, s56
  s_add_u32 s37, s37, s57
  s_sub_i32 s48, s45, 63
  s_max_i32 s48, s48, 0
  s_lshr_b64 s[26:27], s[26:27], 1
  s_sub_u32 s28, s28, 1
  s_cselect_b32 s45, -1, s45
  s_cselect_b32 s48, -1, s48
  s_cselect_b32 s47, 0, s47
  s_mov_b32 s46, 0
  s_xor_b32 s51, s51, 32768
  s_lshl_b32 s57, s24, 15
  s_add_u32 s57, s57, 32768
  s_mov_b32 s85, 130048
  v_xor_b32 v10, 32, v9
  s_getpc_b64 s[64:65]
.Lqa_code_%=:
  s_and_b32 s65, s65, 0xffff
  s_mov_b32 s66, .Lqa_end_%=-.Lqa_code_%=
  s_mov_b32 s67, 0x20000
  v_lshlrev_b32 v18, 3, v82
  s_lshl_b32 s56, s24, 13
  buffer_load_dword v19, v18, s[64:67], s56 offen
  v_mov_b32 v14, s4
  s_mov_b32 s92, 3
  s_mov_b32 s93, 24576
  s_getpc_b64 s[90:91]
.Lqa_pc_%=:
  s_add_u32 s90, s90, .Lqa_ck_%=-.Lqa_pc_%=
  s_addc_u32 s91, s91, 0
.Lqa_dsub_%=:
  s_cmp_ge_u32 s92, 12
  s_cbranch_scc1 .Lqa_dskip_%=
  s_mov_b32 s94, s92
  s_lshr_b32 m0, s94, 1
  s_and_b32 m0, m0, 6
  s_and_b32 s56, s94, 3
  s_lshl_b32 s56, s56, 20
  s_movrels_b64 s[72:73], s[76:77]
  s_add_u32 s56, s56, s25
  s_add_u32 m0, s57, s93
  s_waitcnt vmcnt(18) lgkmcnt(0)
  s_barrier
  s_cmp_lg_u32 s92, 5
  s_cbranch_scc1 .Lqa_dgo_%=
  s_add_u32 s7, s7, 16
  v_mov_b32 v16, s7
  v_mov_b32 v17, -1
  s_mov_b64 exec, 1
  buffer_store_dwordx2 v[16:17], off, s[20:23], s6 sc1
  s_mov_b64 exec, -1
.Lqa_dgo_%=:
  buffer_load_dword v14, off, s[20:23], s5 sc1
  buffer_load_dwordx4 v8, s[72:75], s56 offen offset:0 )ASM" WPOL R"ASM( lds
  s_add_u32 s56, s56, s85
  buffer_load_dwordx4 v8, s[72:75], s56 offen offset:1024 )ASM" WPOL R"ASM( lds
  s_add_u32 s56, s56, s85
  buffer_load_dwordx4 v8, s[72:75], s56 offen offset:2048 )ASM" WPOL R"ASM( lds
  s_add_u32 s56, s56, s85
  buffer_load_dwordx4 v8, s[72:75], s56 offen offset:3072 )ASM" WPOL R"ASM( lds
  s_add_u32 m0, m0, 4096
  s_add_u32 s56, s56, 4096
  s_add_u32 s56, s56, s85
  buffer_load_dwordx4 v8, s[72:75], s56 offen offset:0 )ASM" WPOL R"ASM( lds
  s_add_u32 s56, s56, s85
  buffer_load_dwordx4 v8, s[72:75], s56 offen offset:1024 )ASM" WPOL R"ASM( lds
  s_add_u32 s56, s56, s85
  buffer_load_dwordx4 v8, s[72:75], s56 offen offset:2048 )ASM" WPOL R"ASM( lds
  s_add_u32 s56, s56, s85
  buffer_load_dwordx4 v8, s[72:75], s56 offen offset:3072 )ASM" WPOL R"ASM( lds
.Lqa_dnext_%=:
  s_add_u32 s93, s93, 8192
  s_and_b32 s93, s93, 24576
  s_add_u32 s92, s92, 1
  s_sub_u32 s94, s92, 4
  s_cbranch_scc1 .Lqa_dsub_%=
  v_add_u32 v11, s93, v9
  v_add_u32 v12, s93, v10
  s_cmp_ge_u32 s94, 12
  s_cbranch_scc1 .Lqa_dexit_%=
  s_lshl2_add_u32 s94, s84, s94
  s_sub_u32 s56, s94, 12
  s_cselect_b32 s94, s94, s56
  s_mul_i32 s94, s94, 132
  s_add_u32 s88, s90, s94
  s_addc_u32 s89, s91, 0
  s_setpc_b64 s[88:89]
.Lqa_dskip_%=:
  s_cmp_gt_u32 s92, 12
  s_cbranch_scc1 .Lqa_dnext_%=
  v_readfirstlane_b32 s56, v14
  s_nop 3
  s_cmp_lg_u32 s56, s4
  s_cselect_b32 s47, s47, 0
  s_cselect_b32 s96, 0, 1
  s_mov_b32 s97, s57
  s_cmp_ge_u32 s45, 64
  s_cselect_b32 s57, s1, s30
  s_lshl_b32 s56, s45, 16
  s_add_u32 s56, s56, s57
  s_lshr_b32 s57, s56, 8
  s_and_b32 s57, s57, 0x2000
  s_xor_b32 s56, s56, s57
  s_or_b32 s46, s46, s47
  s_cmp_lg_u32 s47, 0
  s_cselect_b32 s50, s56, 0xf0000000
  s_cselect_b32 m0, s51, 0x30000
  s_cselect_b32 s48, -1, s48
  s_mov_b32 s47, 0
  buffer_load_dwordx4 v82, s[20:23], s50 offen offset:0 sc1 lds
  buffer_load_dwordx4 v82, s[20:23], s50 offen offset:1024 sc1 lds
  buffer_load_dwordx4 v82, s[20:23], s50 offen offset:2048 sc1 lds
  buffer_load_dwordx4 v82, s[20:23], s50 offen offset:3072 sc1 lds
  s_add_u32 m0, m0, 4096
  s_add_u32 s50, s50, 4096
  buffer_load_dwordx4 v82, s[20:23], s50 offen offset:0 sc1 lds
  buffer_load_dwordx4 v82, s[20:23], s50 offen offset:1024 sc1 lds
  buffer_load_dwordx4 v82, s[20:23], s50 offen offset:2048 sc1 lds
  buffer_load_dwordx4 v82, s[20:23], s50 offen offset:3072 sc1 lds
  s_mov_b32 s57, s97
  s_mov_b32 s47, s96
  s_waitcnt vmcnt(8)
  s_barrier
  s_branch .Lqa_dnext_%=
.Lqa_ck_%=:
  ds_read_b64_tr_b16 a[128:129], v11 offset:0
  ds_read_b64_tr_b16 a[192:193], v12 offset:0
  ds_read_b64_tr_b16 a[132:133], v11 offset:1024
  ds_read_b64_tr_b16 a[196:197], v12 offset:1024
  ds_read_b64_tr_b16 a[136:137], v11 offset:2048
  ds_read_b64_tr_b16 a[200:201], v12 offset:2048
  ds_read_b64_tr_b16 a[140:141], v11 offset:3072
  ds_read_b64_tr_b16 a[204:205], v12 offset:3072
  ds_read_b64_tr_b16 a[130:131], v11 offset:4096
  ds_read_b64_tr_b16 a[194:195], v12 offset:4096
  ds_read_b64_tr_b16 a[134:135], v11 offset:5120
  ds_read_b64_tr_b16 a[198:199], v12 offset:5120
  ds_read_b64_tr_b16 a[138:139], v11 offset:6144
  ds_read_b64_tr_b16 a[202:203], v12 offset:6144
  ds_read_b64_tr_b16 a[142:143], v11 offset:7168
  ds_read_b64_tr_b16 a[206:207], v12 offset:7168
  s_branch .Lqa_dsub_%=
  ds_read_b64_tr_b16 a[144:145], v11 offset:0
  ds_read_b64_tr_b16 a[208:209], v12 offset:0
  ds_read_b64_tr_b16 a[148:149], v11 offset:1024
  ds_read_b64_tr_b16 a[212:213], v12 offset:1024
  ds_read_b64_tr_b16 a[152:153], v11 offset:2048
  ds_read_b64_tr_b16 a[216:217], v12 offset:2048
  ds_read_b64_tr_b16 a[156:157], v11 offset:3072
  ds_read_b64_tr_b16 a[220:221], v12 offset:3072
  ds_read_b64_tr_b16 a[146:147], v11 offset:4096
  ds_read_b64_tr_b16 a[210:211], v12 offset:4096
  ds_read_b64_tr_b16 a[150:151], v11 offset:5120
  ds_read_b64_tr_b16 a[214:215], v12 offset:5120
  ds_read_b64_tr_b16 a[154:155], v11 offset:6144
  ds_read_b64_tr_b16 a[218:219], v12 offset:6144
  ds_read_b64_tr_b16 a[158:159], v11 offset:7168
  ds_read_b64_tr_b16 a[222:223], v12 offset:7168
  s_branch .Lqa_dsub_%=
  ds_read_b64_tr_b16 a[160:161], v11 offset:0
  ds_read_b64_tr_b16 a[224:225], v12 offset:0
  ds_read_b64_tr_b16 a[164:165], v11 offset:1024
  ds_read_b64_tr_b16 a[228:229], v12 offset:1024
  ds_read_b64_tr_b16 a[168:169], v11 offset:2048
  ds_read_b64_tr_b16 a[232:233], v12 offset:2048
  ds_read_b64_tr_b16 a[172:173], v11 offset:3072
  ds_read_b64_tr_b16 a[236:237], v12 offset:3072
  ds_read_b64_tr_b16 a[162:163], v11 offset:4096
  ds_read_b64_tr_b16 a[226:227], v12 offset:4096
  ds_read_b64_tr_b16 a[166:167], v11 offset:5120
  ds_read_b64_tr_b16 a[230:231], v12 offset:5120
  ds_read_b64_tr_b16 a[170:171], v11 offset:6144
  ds_read_b64_tr_b16 a[234:235], v12 offset:6144
  ds_read_b64_tr_b16 a[174:175], v11 offset:7168
  ds_read_b64_tr_b16 a[238:239], v12 offset:7168
  s_branch .Lqa_dsub_%=
  ds_read_b64_tr_b16 a[176:177], v11 offset:0
  ds_read_b64_tr_b16 a[240:241], v12 offset:0
  ds_read_b64_tr_b16 a[180:181], v11 offset:1024
  ds_read_b64_tr_b16 a[244:245], v12 offset:1024
  ds_read_b64_tr_b16 a[184:185], v11 offset:2048
  ds_read_b64_tr_b16 a[248:249], v12 offset:2048
  ds_read_b64_tr_b16 a[188:189], v11 offset:3072
  ds_read_b64_tr_b16 a[252:253], v12 offset:3072
  ds_read_b64_tr_b16 a[178:179], v11 offset:4096
  ds_read_b64_tr_b16 a[242:243], v12 offset:4096
  ds_read_b64_tr_b16 a[182:183], v11 offset:5120
  ds_read_b64_tr_b16 a[246:247], v12 offset:5120
  ds_read_b64_tr_b16 a[186:187], v11 offset:6144
  ds_read_b64_tr_b16 a[250:251], v12 offset:6144
  ds_read_b64_tr_b16 a[190:191], v11 offset:7168
  ds_read_b64_tr_b16 a[254:255], v12 offset:7168
  s_branch .Lqa_dsub_%=
  ds_read_b64_tr_b16 v[128:129], v11 offset:0
  ds_read_b64_tr_b16 v[192:193], v12 offset:0
  ds_read_b64_tr_b16 v[132:133], v11 offset:1024
  ds_read_b64_tr_b16 v[196:197], v12 offset:1024
  ds_read_b64_tr_b16 v[136:137], v11 offset:2048
  ds_read_b64_tr_b16 v[200:201], v12 offset:2048
  ds_read_b64_tr_b16 v[140:141], v11 offset:3072
  ds_read_b64_tr_b16 v[204:205], v12 offset:3072
  ds_read_b64_tr_b16 v[130:131], v11 offset:4096
  ds_read_b64_tr_b16 v[194:195], v12 offset:4096
  ds_read_b64_tr_b16 v[134:135], v11 offset:5120
  ds_read_b64_tr_b16 v[198:199], v12 offset:5120
  ds_read_b64_tr_b16 v[138:139], v11 offset:6144
  ds_read_b64_tr_b16 v[202:203], v12 offset:6144
  ds_read_b64_tr_b16 v[142:143], v11 offset:7168
  ds_read_b64_tr_b16 v[206:207], v12 offset:7168
  s_branch .Lqa_dsub_%=
  ds_read_b64_tr_b16 v[144:145], v11 offset:0
  ds_read_b64_tr_b16 v[208:209], v12 offset:0
  ds_read_b64_tr_b16 v[148:149], v11 offset:1024
  ds_read_b64_tr_b16 v[212:213], v12 offset:1024
  ds_read_b64_tr_b16 v[152:153], v11 offset:2048
  ds_read_b64_tr_b16 v[216:217], v12 offset:2048
  ds_read_b64_tr_b16 v[156:157], v11 offset:3072
  ds_read_b64_tr_b16 v[220:221], v12 offset:3072
  ds_read_b64_tr_b16 v[146:147], v11 offset:4096
  ds_read_b64_tr_b16 v[210:211], v12 offset:4096
  ds_read_b64_tr_b16 v[150:151], v11 offset:5120
  ds_read_b64_tr_b16 v[214:215], v12 offset:5120
  ds_read_b64_tr_b16 v[154:155], v11 offset:6144
  ds_read_b64_tr_b16 v[218:219], v12 offset:6144
  ds_read_b64_tr_b16 v[158:159], v11 offset:7168
  ds_read_b64_tr_b16 v[222:223], v12 offset:7168
  s_branch .Lqa_dsub_%=
  ds_read_b64_tr_b16 v[160:161], v11 offset:0
  ds_read_b64_tr_b16 v[224:225], v12 offset:0
  ds_read_b64_tr_b16 v[164:165], v11 offset:1024
  ds_read_b64_tr_b16 v[228:229], v12 offset:1024
  ds_read_b64_tr_b16 v[168:169], v11 offset:2048
  ds_read_b64_tr_b16 v[232:233], v12 offset:2048
  ds_read_b64_tr_b16 v[172:173], v11 offset:3072
  ds_read_b64_tr_b16 v[236:237], v12 offset:3072
  ds_read_b64_tr_b16 v[162:163], v11 offset:4096
  ds_read_b64_tr_b16 v[226:227], v12 offset:4096
  ds_read_b64_tr_b16 v[166:167], v11 offset:5120
  ds_read_b64_tr_b16 v[230:231], v12 offset:5120
  ds_read_b64_tr_b16 v[170:171], v11 offset:6144
  ds_read_b64_tr_b16 v[234:235], v12 offset:6144
  ds_read_b64_tr_b16 v[174:175], v11 offset:7168
  ds_read_b64_tr_b16 v[238:239], v12 offset:7168
  s_branch .Lqa_dsub_%=
  ds_read_b64_tr_b16 v[176:177], v11 offset:0
  ds_read_b64_tr_b16 v[240:241], v12 offset:0
  ds_read_b64_tr_b16 v[180:181], v11 offset:1024
  ds_read_b64_tr_b16 v[244:245], v12 offset:1024
  ds_read_b64_tr_b16 v[184:185], v11 offset:2048
  ds_read_b64_tr_b16 v[248:249], v12 offset:2048
  ds_read_b64_tr_b16 v[188:189], v11 offset:3072
  ds_read_b64_tr_b16 v[252:253], v12 offset:3072
  ds_read_b64_tr_b16 v[178:179], v11 offset:4096
  ds_read_b64_tr_b16 v[242:243], v12 offset:4096
  ds_read_b64_tr_b16 v[182:183], v11 offset:5120
  ds_read_b64_tr_b16 v[246:247], v12 offset:5120
  ds_read_b64_tr_b16 v[186:187], v11 offset:6144
  ds_read_b64_tr_b16 v[250:251], v12 offset:6144
  ds_read_b64_tr_b16 v[190:191], v11 offset:7168
  ds_read_b64_tr_b16 v[254:255], v12 offset:7168
  s_branch .Lqa_dsub_%=
  ds_read_b64_tr_b16 a[0:1], v11 offset:0
  ds_read_b64_tr_b16 a[64:65], v12 offset:0
  ds_read_b64_tr_b16 a[4:5], v11 offset:1024
  ds_read_b64_tr_b16 a[68:69], v12 offset:1024
  ds_read_b64_tr_b16 a[8:9], v11 offset:2048
  ds_read_b64_tr_b16 a[72:73], v12 offset:2048
  ds_read_b64_tr_b16 a[12:13], v11 offset:3072
  ds_read_b64_tr_b16 a[76:77], v12 offset:3072
  ds_read_b64_tr_b16 a[2:3], v11 offset:4096
  ds_read_b64_tr_b16 a[66:67], v12 offset:4096
  ds_read_b64_tr_b16 a[6:7], v11 offset:5120
  ds_read_b64_tr_b16 a[70:71], v12 offset:5120
  ds_read_b64_tr_b16 a[10:11], v11 offset:6144
  ds_read_b64_tr_b16 a[74:75], v12 offset:6144
  ds_read_b64_tr_b16 a[14:15], v11 offset:7168
  ds_read_b64_tr_b16 a[78:79], v12 offset:7168
  s_branch .Lqa_dsub_%=
  ds_read_b64_tr_b16 a[16:17], v11 offset:0
  ds_read_b64_tr_b16 a[80:81], v12 offset:0
  ds_read_b64_tr_b16 a[20:21], v11 offset:1024
  ds_read_b64_tr_b16 a[84:85], v12 offset:1024
  ds_read_b64_tr_b16 a[24:25], v11 offset:2048
  ds_read_b64_tr_b16 a[88:89], v12 offset:2048
  ds_read_b64_tr_b16 a[28:29], v11 offset:3072
  ds_read_b64_tr_b16 a[92:93], v12 offset:3072
  ds_read_b64_tr_b16 a[18:19], v11 offset:4096
  ds_read_b64_tr_b16 a[82:83], v12 offset:4096
  ds_read_b64_tr_b16 a[22:23], v11 offset:5120
  ds_read_b64_tr_b16 a[86:87], v12 offset:5120
  ds_read_b64_tr_b16 a[26:27], v11 offset:6144
  ds_read_b64_tr_b16 a[90:91], v12 offset:6144
  ds_read_b64_tr_b16 a[30:31], v11 offset:7168
  ds_read_b64_tr_b16 a[94:95], v12 offset:7168
  s_branch .Lqa_dsub_%=
  ds_read_b64_tr_b16 a[32:33], v11 offset:0
  ds_read_b64_tr_b16 a[96:97], v12 offset:0
  ds_read_b64_tr_b16 a[36:37], v11 offset:1024
  ds_read_b64_tr_b16 a[100:101], v12 offset:1024
  ds_read_b64_tr_b16 a[40:41], v11 offset:2048
  ds_read_b64_tr_b16 a[104:105], v12 offset:2048
  ds_read_b64_tr_b16 a[44:45], v11 offset:3072
  ds_read_b64_tr_b16 a[108:109], v12 offset:3072
  ds_read_b64_tr_b16 a[34:35], v11 offset:4096
  ds_read_b64_tr_b16 a[98:99], v12 offset:4096
  ds_read_b64_tr_b16 a[38:39], v11 offset:5120
  ds_read_b64_tr_b16 a[102:103], v12 offset:5120
  ds_read_b64_tr_b16 a[42:43], v11 offset:6144
  ds_read_b64_tr_b16 a[106:107], v12 offset:6144
  ds_read_b64_tr_b16 a[46:47], v11 offset:7168
  ds_read_b64_tr_b16 a[110:111], v12 offset:7168
  s_branch .Lqa_dsub_%=
  ds_read_b64_tr_b16 a[48:49], v11 offset:0
  ds_read_b64_tr_b16 a[112:113], v12 offset:0
  ds_read_b64_tr_b16 a[52:53], v11 offset:1024
  ds_read_b64_tr_b16 a[116:117], v12 offset:1024
  ds_read_b64_tr_b16 a[56:57], v11 offset:2048
  ds_read_b64_tr_b16 a[120:121], v12 offset:2048
  ds_read_b64_tr_b16 a[60:61], v11 offset:3072
  ds_read_b64_tr_b16 a[124:125], v12 offset:3072
  ds_read_b64_tr_b16 a[50:51], v11 offset:4096
  ds_read_b64_tr_b16 a[114:115], v12 offset:4096
  ds_read_b64_tr_b16 a[54:55], v11 offset:5120
  ds_read_b64_tr_b16 a[118:119], v12 offset:5120
  ds_read_b64_tr_b16 a[58:59], v11 offset:6144
  ds_read_b64_tr_b16 a[122:123], v12 offset:6144
  ds_read_b64_tr_b16 a[62:63], v11 offset:7168
  ds_read_b64_tr_b16 a[126:127], v12 offset:7168
  s_branch .Lqa_dsub_%=
.Lqa_dexit_%=:
  s_waitcnt lgkmcnt(0)
  .if DBGT
  s_memrealtime s[72:73]
  s_waitcnt lgkmcnt(0)
  v_mov_b32 v8, s72
  s_mov_b64 exec, 1
  buffer_store_dword v8, off, s[20:23], s69 offset:60
  s_mov_b64 exec, -1
  .endif
  s_branch .Lqa_go1_%=
.Lqa_go0_%=:
  s_and_b32 vcc_lo, s55, 15
  s_cbranch_scc1 .Lqa_go2_%=
  s_add_u32 s7, s7, 16
  v_mov_b32 v14, s7
  v_mov_b32 v15, -1
  s_mov_b64 exec, 1
  buffer_store_dwordx2 v[14:15], off, s[20:23], s6 sc1
  s_mov_b64 exec, -1
.Lqa_go2_%=:
  s_add_u32 s55, s55, 1
  s_cmp_gt_u32 s55, 0x40000
  s_cbranch_scc1 .Lqa_end_%=
  s_sleep 2
  buffer_load_dword v14, off, s[20:23], s5 sc1
.Lqa_go1_%=:
  s_waitcnt vmcnt(0)
  v_readfirstlane_b32 vcc_lo, v14
  s_nop 3
  s_cmp_eq_u32 vcc_lo, s4
  s_cbranch_scc1 .Lqa_go0_%=
  s_mov_b32 s4, vcc_lo
  s_bitcmp0_b32 s4, 2
  s_cbranch_scc1 .Lqa_go3_%=
  s_lshl_b32 s2, s24, 6
  v_mbcnt_lo_u32_b32 v0, -1, s2
  v_mbcnt_hi_u32_b32 v0, -1, v0
  s_mov_b64 s[0:1], s[8:9]
  s_mov_b32 s2, s10
  s_waitcnt vmcnt(0) lgkmcnt(0)
  s_barrier
  s_branch _ZN3rmt11main_kernelENS_6ParamsE
.Lqa_go3_%=:
  s_lshl_b32 s56, s4, 13
  s_and_b32 s56, s56, 0x2000
  s_lshl_b32 s57, s4, 12
  s_and_b32 s57, s57, 0x2000
  s_xor_b32 s1, s1, s56
  s_xor_b32 s31, s31, s56
  s_xor_b32 s32, s32, s56
  s_xor_b32 s33, s33, s56
  s_xor_b32 s34, s34, s56
  s_xor_b32 s35, s35, s56
  s_xor_b32 s2, s2, s56
  s_xor_b32 s98, s98, s57
  v_cmp_ne_u32 s[4:5], 0, v0
  v_cmp_ne_u32 s[6:7], 0, v1
  v_cmp_ne_u32 s[8:9], 0, v2
  v_cmp_ne_u32 s[10:11], 0, v3
  v_cmp_ne_u32 s[12:13], 0, v4
  v_cmp_ne_u32 s[14:15], 0, v5
  v_cmp_ne_u32 s[16:17], 0, v6
  v_cmp_ne_u32 s[18:19], 0, v7
  s_mov_b32 exec_hi, 15
  v_xor_b32 v83, s56, v83
  s_not_b64 exec, exec
  v_xor_b32 v83, s57, v83
  s_mov_b64 exec, -1
  .if DBGT
  s_mov_b32 s70, s86
  .endif
  s_branch .Lqa_idle2_%=
.Lqa_abort_%=:
  v_mov_b32 v88, 1
  buffer_store_dword v88, v87, s[20:23], 0 offen offset:0 sc1
  s_branch .Lqa_end_%=
.Lqa_fin_%=:
  .if DBGT
  s_memrealtime s[72:73]
  s_lshl_b32 s74, s49, 2
  s_or_b32 s74, s74, 1
  s_waitcnt lgkmcnt(0)
  s_lshl_b32 s72, s72, 10
  s_or_b32 s72, s72, s74
  v_mov_b32 v88, s72
  buffer_store_dword v88, v87, s[20:23], s70 offen
  s_add_u32 s70, s70, 4
  .endif
  s_and_b32 s56, s49, 1
  s_cmp_ge_u32 s49, 64
  s_addc_u32 s56, s56, s56
  s_mul_i32 s56, s56, 24576
  s_add_u32 s56, s56, 65536
  v_add_u32 v126, s56, v82
  v_add_u32 v122, s59, v126
  ds_read_b128 v[90:93], v122 offset:0
  ds_read_b128 v[94:97], v122 offset:1024
  ds_read_b128 v[98:101], v122 offset:2048
  ds_read_b128 v[102:105], v122 offset:3072
  ds_read_b128 v[106:109], v126 offset:8192
  ds_read_b128 v[110:113], v126 offset:9216
  ds_read_b128 v[114:117], v126 offset:10240
  ds_read_b128 v[118:121], v126 offset:11264
  v_mov_b32 v7, 0xff800000
  v_cndmask_b32_e64 v0, 0, v7, s[4:5]
  v_cndmask_b32_e64 v1, 0, v7, s[6:7]
  v_cndmask_b32_e64 v2, 0, v7, s[8:9]
  v_cndmask_b32_e64 v3, 0, v7, s[10:11]
  v_cndmask_b32_e64 v4, 0, v7, s[12:13]
  v_cndmask_b32_e64 v5, 0, v7, s[14:15]
  v_cndmask_b32_e64 v6, 0, v7, s[16:17]
  v_cndmask_b32_e64 v7, 0, v7, s[18:19]
  v_add_u32 v127, s60, v126
  s_and_b32 s57, s49, 63
  s_lshl_b32 s57, s57, 19
  s_lshr_b32 s56, s57, 8
  s_and_b32 s56, s56, 0x2000
  s_add_u32 s57, s57, s33
  s_xor_b32 s57, s57, s56
  s_cmp_ge_u32 s49, 64
  s_cselect_b32 s53, s2, s35
  s_cselect_b32 s56, s3, s29
  s_cselect_b32 s62, 0, 3
  s_cselect_b32 s63, 3, 0
  s_add_u32 s38, s38, s62
  s_add_u32 s39, s39, s63
  s_and_b32 s54, s49, 63
  s_sub_u32 s54, s54, s56
  s_lshl_b32 s54, s54, 1
  s_add_u32 s54, s54, 6
  s_sub_u32 s42, s38, -1
  s_cmp_lt_u32 s38, s40
  s_cselect_b32 s42, s42, -1
  s_sub_u32 s43, s39, 63
  s_cmp_lt_u32 s39, s41
  s_cselect_b32 s43, s43, -1
  s_waitcnt lgkmcnt(7)
  v_mfma_f32_16x16x32_bf16 v[122:125], v[48:51], v[90:93], 0
  ds_read_b128 v[48:51], v126 offset:12288
  s_waitcnt lgkmcnt(7)
  v_mfma_f32_16x16x32_bf16 v[122:125], v[52:55], v[94:97], v[122:125]
  ds_read_b128 v[52:55], v126 offset:13312
  s_waitcnt lgkmcnt(7)
  v_mfma_f32_16x16x32_bf16 v[122:125], v[56:59], v[98:101], v[122:125]
  ds_read_b128 v[56:59], v126 offset:14336
  s_waitcnt lgkmcnt(7)
  v_mfma_f32_16x16x32_bf16 v[122:125], v[60:63], v[102:105], v[122:125]
  ds_read_b128 v[60:63], v126 offset:15360
  s_waitcnt lgkmcnt(7)
  v_mfma_f32_16x16x32_bf16 v[0:3], v[106:109], v[90:93], v[0:3]
  ds_read_b128 v[106:109], v127 offset:16384
  s_waitcnt lgkmcnt(7)
  v_mfma_f32_16x16x32_bf16 v[0:3], v[110:113], v[94:97], v[0:3]
  ds_read_b128 v[110:113], v127 offset:20480
  s_waitcnt lgkmcnt(7)
  v_mfma_f32_16x16x32_bf16 v[0:3], v[114:117], v[98:101], v[0:3]
  ds_read_b128 v[114:117], v127 offset:17408
  s_waitcnt lgkmcnt(7)
  v_mfma_f32_16x16x32_bf16 v[0:3], v[118:121], v[102:105], v[0:3]
  ds_read_b128 v[118:121], v127 offset:21504
  s_waitcnt lgkmcnt(7)
  v_mfma_f32_16x16x32_bf16 v[4:7], v[48:51], v[90:93], v[4:7]
  s_waitcnt lgkmcnt(6)
  v_mfma_f32_16x16x32_bf16 v[4:7], v[52:55], v[94:97], v[4:7]
  s_waitcnt lgkmcnt(5)
  v_mfma_f32_16x16x32_bf16 v[4:7], v[56:59], v[98:101], v[4:7]
  v_max_f32 v126, v122, v124
  s_waitcnt lgkmcnt(4)
  v_mfma_f32_16x16x32_bf16 v[4:7], v[60:63], v[102:105], v[4:7]
  v_mov_b32 v48, 0x3f803f80
  v_mov_b32 v49, 0x3f803f80
  v_max3_f32 v126, v126, v0, v1
  v_max3_f32 v126, v126, v2, v3
  v_mov_b64 v[50:51], v[48:49]
  s_nop 2
  v_max3_f32 v126, v126, v4, v5
  v_max3_f32 v126, v126, v6, v7
  v_mov_b32 v127, v126
  s_nop 1
  v_permlane16_swap_b32 v126, v127
  v_max_f32 v126, v126, v127
  v_mov_b32 v127, v126
  s_nop 1
  v_permlane32_swap_b32 v126, v127
  v_max_f32 v126, v126, v127
  v_mul_f32 v126, s58, v126
  v_fma_f32 v122, v122, s58, -v126
  v_fma_f32 v124, v124, s58, -v126
  v_exp_f32 v122, v122
  v_exp_f32 v124, v124
  v_fma_f32 v0, v0, s58, -v126
  v_fma_f32 v1, v1, s58, -v126
  v_exp_f32 v0, v0
  v_exp_f32 v1, v1
  v_cvt_pk_bf16_f32 v70, v122, v124
  v_cvt_pk_bf16_f32 v75, v122, v124
  v_fma_f32 v2, v2, s58, -v126
  v_fma_f32 v3, v3, s58, -v126
  v_exp_f32 v2, v2
  v_exp_f32 v3, v3
  v_mfma_f32_16x16x32_bf16 v[56:59], v[48:51], v[70:73], 0
  v_mfma_f32_16x16x32_bf16 v[90:93], v[64:67], v[70:73], 0
  v_cvt_pk_bf16_f32 v52, v0, v1
  v_fma_f32 v4, v4, s58, -v126
  v_fma_f32 v5, v5, s58, -v126
  v_exp_f32 v4, v4
  v_exp_f32 v5, v5
  v_mfma_f32_16x16x32_bf16 v[94:97], v[64:67], v[74:77], 0
  v_mfma_f32_16x16x32_bf16 v[98:101], v[64:67], v[68:71], 0
  v_cvt_pk_bf16_f32 v53, v2, v3
  v_fma_f32 v6, v6, s58, -v126
  v_fma_f32 v7, v7, s58, -v126
  v_exp_f32 v6, v6
  v_exp_f32 v7, v7
  v_mfma_f32_16x16x32_bf16 v[102:105], v[64:67], v[72:75], 0
  v_cvt_pk_bf16_f32 v54, v4, v5
  v_cvt_pk_bf16_f32 v55, v6, v7
  s_nop 1
  v_mfma_f32_16x16x32_bf16 v[56:59], v[48:51], v[52:55], v[56:59]
  s_waitcnt lgkmcnt(3)
  v_mfma_f32_16x16x32_bf16 v[90:93], v[106:109], v[52:55], v[90:93]
  s_waitcnt lgkmcnt(2)
  v_mfma_f32_16x16x32_bf16 v[94:97], v[110:113], v[52:55], v[94:97]
  s_waitcnt lgkmcnt(1)
  v_mfma_f32_16x16x32_bf16 v[98:101], v[114:117], v[52:55], v[98:101]
  s_waitcnt lgkmcnt(0)
  v_mfma_f32_16x16x32_bf16 v[102:105], v[118:121], v[52:55], v[102:105]
  v_rcp_f32 v127, v56
  s_nop 0
  v_mul_f32 v90, v90, v127
  v_mul_f32 v91, v91, v127
  v_mul_f32 v92, v92, v127
  v_mul_f32 v93, v93, v127
  v_mul_f32 v94, v94, v127
  v_mul_f32 v95, v95, v127
  v_mul_f32 v96, v96, v127
  v_mul_f32 v97, v97, v127
  v_cvt_pk_bf16_f32 v0, v90, v91
  v_cvt_pk_bf16_f32 v1, v92, v93
  v_cvt_pk_bf16_f32 v2, v94, v95
  v_cvt_pk_bf16_f32 v3, v96, v97
  buffer_store_dwordx4 v[0:3], v82, s[20:23], s57 offen offset:0 sc1
  v_mul_f32 v98, v98, v127
  v_mul_f32 v99, v99, v127
  v_mul_f32 v100, v100, v127
  v_mul_f32 v101, v101, v127
  v_mul_f32 v102, v102, v127
  v_mul_f32 v103, v103, v127
  v_mul_f32 v104, v104, v127
  v_mul_f32 v105, v105, v127
  v_cvt_pk_bf16_f32 v4, v98, v99
  v_cvt_pk_bf16_f32 v5, v100, v101
  v_cvt_pk_bf16_f32 v6, v102, v103
  v_cvt_pk_bf16_f32 v7, v104, v105
  buffer_store_dwordx4 v[4:7], v82, s[20:23], s57 offen offset:1024 sc1
  s_mov_b32 s49, -1
  s_nop 0
  s_setpc_b64 s[66:67]
.Lqa_idle_%=:
  s_add_u32 s55, s55, 1
  s_cmp_gt_u32 s55, 0x40000
  s_cbranch_scc1 .Lqa_abort_%=
  s_sleep SLP
  buffer_load_dword v84, v83, s[20:23], 0 offen offset:0 sc1
  s_waitcnt vmcnt(1)
  v_mov_b32 v88, s54
  buffer_store_dword v88, v87, s[20:23], s53 offen offset:0 sc1
  s_mov_b32 s53, 0xf0000000
  s_waitcnt vmcnt(1)
  v_cmp_gt_u32 vcc, s48, v84
  v_cmp_gt_u32 s[62:63], s42, v84
  v_cmp_gt_u32 s[64:65], s43, v84
  s_cmp_eq_u32 vcc_lo, 0
  s_cselect_b32 s47, 1, 0
  s_and_b32 s56, s65, 240
  s_cselect_b32 s49, -1, s39
  s_and_b32 s56, s63, 15
  s_cselect_b32 s49, s49, s38
  s_mul_i32 s56, s49, 0x40400
  s_and_b32 s56, s56, 0xfffc2000
  s_xor_b32 s61, s56, s32
  s_cmp_lt_i32 s49, 0
  s_cbranch_scc1 .Lqa_idle2_%=
  buffer_load_dwordx4 v[48:51], v85, s[20:23], s61 offen offset:0 sc1
  buffer_load_dwordx4 v[52:55], v85, s[20:23], s61 offen offset:512 sc1
  buffer_load_dwordx4 v[56:59], v85, s[20:23], s61 offen offset:1024 sc1
  buffer_load_dwordx4 v[60:63], v85, s[20:23], s61 offen offset:1536 sc1
  buffer_load_dwordx2 v[64:65], v86, s[20:23], s61 offen offset:0 sc1
  buffer_load_dwordx2 v[66:67], v86, s[20:23], s61 offen offset:512 sc1
.Lqa_idle2_%=:
  s_cmp_eq_u32 s47, 0
  s_cbranch_scc1 .Lqa_idle3_%=
  s_cmp_ge_u32 s45, 64
  s_cselect_b32 s57, s1, s30
  s_lshl_b32 s56, s45, 16
  s_add_u32 s56, s56, s57
  s_lshr_b32 s57, s56, 8
  s_and_b32 s57, s57, 0x2000
  s_xor_b32 s56, s56, s57
  s_or_b32 s46, s46, s47
  s_cmp_lg_u32 s47, 0
  s_cselect_b32 s50, s56, 0xf0000000
  s_cselect_b32 m0, s51, 0x30000
  s_cselect_b32 s48, -1, s48
  s_mov_b32 s47, 0
  buffer_load_dwordx4 v82, s[20:23], s50 offen offset:0 sc1 lds
  buffer_load_dwordx4 v82, s[20:23], s50 offen offset:1024 sc1 lds
  buffer_load_dwordx4 v82, s[20:23], s50 offen offset:2048 sc1 lds
  buffer_load_dwordx4 v82, s[20:23], s50 offen offset:3072 sc1 lds
  s_add_u32 m0, m0, 4096
  s_add_u32 s50, s50, 4096
  buffer_load_dwordx4 v82, s[20:23], s50 offen offset:0 sc1 lds
  buffer_load_dwordx4 v82, s[20:23], s50 offen offset:1024 sc1 lds
  buffer_load_dwordx4 v82, s[20:23], s50 offen offset:2048 sc1 lds
  buffer_load_dwordx4 v82, s[20:23], s50 offen offset:3072 sc1 lds
.Lqa_idle3_%=:
  s_cmp_lt_i32 s49, 0
  s_cbranch_scc1 .Lqa_idle4_%=
  s_waitcnt vmcnt(0)
  s_call_b64 s[66:67], .Lqa_fin_%=
.Lqa_idle4_%=:
  s_cmp_eq_u32 s46, 0
  s_cbranch_scc1 .Lqa_idle5_%=
  s_cmp_ge_u32 s45, 64
  s_cselect_b32 s56, s39, s38
  s_add_u32 s56, s56, 6
  s_cmp_gt_u32 s56, s45
  s_cbranch_scc0 .Lqa_idle5_%=
  s_waitcnt vmcnt(0)
  v_mov_b32 v88, s54
  buffer_store_dword v88, v87, s[20:23], s53 offen offset:0 sc1
  s_mov_b32 s53, 0xf0000000
  s_branch .Lqa_top_%=
.Lqa_idle5_%=:
  s_and_b32 s56, s42, s43
  s_and_b32 s56, s56, s45
  s_and_b32 s56, s56, s53
  s_cmp_lt_i32 s56, 0
  s_cbranch_scc0 .Lqa_idle_%=
  s_branch .Lqa_end_%=
.Lqa_top_%=:
  v_xor_b32 v78, 32768, v78
  v_xor_b32 v79, 32768, v79
  v_xor_b32 v80, 32768, v80
  v_xor_b32 v81, 32768, v81
  s_mov_b32 s44, s45
  s_barrier
  .if DBGT
  s_memrealtime s[72:73]
  s_lshl_b32 s74, s44, 2
  s_or_b32 s74, s74, 0
  s_waitcnt lgkmcnt(0)
  s_lshl_b32 s72, s72, 10
  s_or_b32 s72, s72, s74
  v_mov_b32 v88, s72
  buffer_store_dword v88, v87, s[20:23], s70 offen
  s_add_u32 s70, s70, 4
  .endif
  ds_read_b128 v[16:19], v78 offset:0
  ds_read_b128 v[20:23], v79 offset:0
  ds_read_b128 v[24:27], v80 offset:0
  ds_read_b128 v[28:31], v81 offset:0
  ds_read_b128 v[32:35], v78 offset:256
  s_waitcnt lgkmcnt(4)
  v_mfma_f32_16x16x32_bf16 v[0:3], a[128:131], v[16:19], 0
  ds_read_b128 v[36:39], v79 offset:256
  s_bitcmp1_b32 s26, 0
  v_mfma_f32_16x16x32_bf16 v[4:7], a[192:195], v[16:19], 0
  s_cselect_b32 s45, s37, s36
  s_cselect_b32 s47, 0, 1
  s_waitcnt lgkmcnt(4)
  v_mfma_f32_16x16x32_bf16 v[0:3], a[132:135], v[20:23], v[0:3]
  ds_read_b128 v[16:19], v80 offset:256
  s_cselect_b32 s56, 0, 3
  v_mfma_f32_16x16x32_bf16 v[4:7], a[196:199], v[20:23], v[4:7]
  s_cselect_b32 s57, 3, 0
  s_add_u32 s36, s36, s56
  s_waitcnt lgkmcnt(4)
  v_mfma_f32_16x16x32_bf16 v[0:3], a[136:139], v[24:27], v[0:3]
  ds_read_b128 v[20:23], v81 offset:256
  s_add_u32 s37, s37, s57
  v_mfma_f32_16x16x32_bf16 v[4:7], a[200:203], v[24:27], v[4:7]
  s_sub_i32 s48, s45, 63
  s_max_i32 s48, s48, 0
  s_waitcnt lgkmcnt(4)
  v_mfma_f32_16x16x32_bf16 v[0:3], a[140:143], v[28:31], v[0:3]
  ds_read_b128 v[24:27], v78 offset:512
  s_lshr_b64 s[26:27], s[26:27], 1
  v_mfma_f32_16x16x32_bf16 v[4:7], a[204:207], v[28:31], v[4:7]
  s_sub_u32 s28, s28, 1
  s_cselect_b32 s45, -1, s45
  s_waitcnt lgkmcnt(4)
  v_mfma_f32_16x16x32_bf16 v[0:3], a[144:147], v[32:35], v[0:3]
  ds_read_b128 v[28:31], v79 offset:512
  s_cselect_b32 s48, -1, s48
  v_mfma_f32_16x16x32_bf16 v[4:7], a[208:211], v[32:35], v[4:7]
  s_cselect_b32 s47, 0, s47
  s_mov_b32 s46, 0
  s_waitcnt lgkmcnt(4)
  v_mfma_f32_16x16x32_bf16 v[0:3], a[148:151], v[36:39], v[0:3]
  ds_read_b128 v[32:35], v80 offset:512
  s_xor_b32 s51, s51, 32768
  s_waitcnt vmcnt(2)
  v_mfma_f32_16x16x32_bf16 v[4:7], a[212:215], v[36:39], v[4:7]
  v_cmp_gt_u32 vcc, s48, v84
  v_cmp_gt_u32 s[62:63], s42, v84
  s_cmp_eq_u32 vcc_lo, 0
  s_waitcnt lgkmcnt(4)
  v_mfma_f32_16x16x32_bf16 v[0:3], a[152:155], v[16:19], v[0:3]
  ds_read_b128 v[36:39], v81 offset:512
  s_cselect_b32 s47, 1, 0
  s_and_b32 s56, s63, 15
  v_mfma_f32_16x16x32_bf16 v[4:7], a[216:219], v[16:19], v[4:7]
  s_cselect_b32 s49, -1, s38
  s_mul_i32 s56, s49, 0x40400
  s_and_b32 s56, s56, 0xfffc2000
  s_waitcnt lgkmcnt(4)
  v_mfma_f32_16x16x32_bf16 v[0:3], a[156:159], v[20:23], v[0:3]
  ds_read_b128 v[16:19], v78 offset:768
  s_xor_b32 s61, s56, s32
  s_cmp_lt_i32 s49, 0
  v_mfma_f32_16x16x32_bf16 v[4:7], a[220:223], v[20:23], v[4:7]
  s_waitcnt lgkmcnt(4)
  v_mfma_f32_16x16x32_bf16 v[0:3], a[160:163], v[24:27], v[0:3]
  ds_read_b128 v[20:23], v79 offset:768
  v_mfma_f32_16x16x32_bf16 v[4:7], a[224:227], v[24:27], v[4:7]
  s_cbranch_scc0 .Lqa_job0_%=
  s_waitcnt lgkmcnt(4)
  v_mfma_f32_16x16x32_bf16 v[0:3], a[164:167], v[28:31], v[0:3]
  ds_read_b128 v[24:27], v80 offset:768
  v_mfma_f32_16x16x32_bf16 v[4:7], a[228:231], v[28:31], v[4:7]
  s_and_b32 s56, s44, 1
  s_cmp_ge_u32 s44, 64
  s_waitcnt lgkmcnt(4)
  v_mfma_f32_16x16x32_bf16 v[0:3], a[168:171], v[32:35], v[0:3]
  ds_read_b128 v[28:31], v81 offset:768
  s_addc_u32 s56, s56, s56
  v_mfma_f32_16x16x32_bf16 v[4:7], a[232:235], v[32:35], v[4:7]
  s_mul_i32 s56, s56, 24576
  s_add_u32 s56, s56, 65536
  s_waitcnt lgkmcnt(4)
  v_mfma_f32_16x16x32_bf16 v[0:3], a[172:175], v[36:39], v[0:3]
  ds_read_b128 v[32:35], v78 offset:16384
  s_add_u32 s56, s56, s68
  v_mfma_f32_16x16x32_bf16 v[4:7], a[236:239], v[36:39], v[4:7]
  v_add_u32 v89, s56, v82
  s_and_b32 s52, s44, 63
  s_waitcnt lgkmcnt(4)
  v_mfma_f32_16x16x32_bf16 v[0:3], a[176:179], v[16:19], v[0:3]
  ds_read_b128 v[36:39], v79 offset:16384
  s_lshl_b32 s52, s52, 20
  v_mfma_f32_16x16x32_bf16 v[4:7], a[240:243], v[16:19], v[4:7]
  s_lshr_b32 s56, s52, 8
  s_and_b32 s56, s56, 0x2000
  s_waitcnt lgkmcnt(4)
  v_mfma_f32_16x16x32_bf16 v[0:3], a[180:183], v[20:23], v[0:3]
  ds_read_b128 v[16:19], v80 offset:16384
  s_add_u32 s52, s52, s31
  v_mfma_f32_16x16x32_bf16 v[4:7], a[244:247], v[20:23], v[4:7]
  s_xor_b32 s52, s52, s56
  s_waitcnt lgkmcnt(4)
  v_mfma_f32_16x16x32_bf16 v[0:3], a[184:187], v[24:27], v[0:3]
  ds_read_b128 v[20:23], v81 offset:16384
  v_mfma_f32_16x16x32_bf16 v[4:7], a[248:251], v[24:27], v[4:7]
  s_waitcnt lgkmcnt(4)
  v_mfma_f32_16x16x32_bf16 v[0:3], a[188:191], v[28:31], v[0:3]
  ds_read_b128 v[24:27], v78 offset:16640
  v_mfma_f32_16x16x32_bf16 v[4:7], a[252:255], v[28:31], v[4:7]
  s_waitcnt lgkmcnt(4)
  v_mfma_f32_16x16x32_bf16 v[8:11], a[128:131], v[32:35], 0
  ds_read_b128 v[28:31], v79 offset:16640
  v_mfma_f32_16x16x32_bf16 v[12:15], a[192:195], v[32:35], 0
  s_waitcnt lgkmcnt(4)
  v_mfma_f32_16x16x32_bf16 v[8:11], a[132:135], v[36:39], v[8:11]
  ds_read_b128 v[32:35], v80 offset:16640
  v_mfma_f32_16x16x32_bf16 v[12:15], a[196:199], v[36:39], v[12:15]
  s_waitcnt lgkmcnt(4)
  v_mfma_f32_16x16x32_bf16 v[8:11], a[136:139], v[16:19], v[8:11]
  ds_read_b128 v[36:39], v81 offset:16640
  v_mfma_f32_16x16x32_bf16 v[12:15], a[200:203], v[16:19], v[12:15]
  buffer_load_dword v84, v83, s[20:23], 0 offen offset:0 sc1
  s_waitcnt lgkmcnt(4)
  v_mfma_f32_16x16x32_bf16 v[8:11], a[140:143], v[20:23], v[8:11]
  ds_read_b128 v[16:19], v78 offset:16896
  v_cvt_pk_bf16_f32 v40, v0, v1
  v_mfma_f32_16x16x32_bf16 v[12:15], a[204:207], v[20:23], v[12:15]
  v_cvt_pk_bf16_f32 v41, v2, v3
  v_cvt_pk_bf16_f32 v42, v4, v5
  s_waitcnt lgkmcnt(4)
  v_mfma_f32_16x16x32_bf16 v[8:11], a[144:147], v[24:27], v[8:11]
  ds_read_b128 v[20:23], v79 offset:16896
  v_cvt_pk_bf16_f32 v43, v6, v7
  v_mfma_f32_16x16x32_bf16 v[12:15], a[208:211], v[24:27], v[12:15]
  buffer_store_dwordx4 v[40:43], v82, s[20:23], s52 offen offset:0 sc1
  ds_write_b128 v89, v[40:43] offset:8192
  s_waitcnt lgkmcnt(5)
  v_mfma_f32_16x16x32_bf16 v[8:11], a[148:151], v[28:31], v[8:11]
  ds_read_b128 v[24:27], v80 offset:16896
  v_mfma_f32_16x16x32_bf16 v[12:15], a[212:215], v[28:31], v[12:15]
  s_waitcnt lgkmcnt(5)
  v_mfma_f32_16x16x32_bf16 v[8:11], a[152:155], v[32:35], v[8:11]
  ds_read_b128 v[28:31], v81 offset:16896
  v_mfma_f32_16x16x32_bf16 v[12:15], a[216:219], v[32:35], v[12:15]
  s_waitcnt vmcnt(2)
  v_mov_b32 v88, s54
  s_waitcnt lgkmcnt(5)
  v_mfma_f32_16x16x32_bf16 v[8:11], a[156:159], v[36:39], v[8:11]
  ds_read_b128 v[32:35], v78 offset:17152
  buffer_store_dword v88, v87, s[20:23], s53 offen offset:0 sc1
  v_mfma_f32_16x16x32_bf16 v[12:15], a[220:223], v[36:39], v[12:15]
  s_mov_b32 s53, 0xf0000000
  s_waitcnt lgkmcnt(5)
  v_mfma_f32_16x16x32_bf16 v[8:11], a[160:163], v[16:19], v[8:11]
  ds_read_b128 v[36:39], v79 offset:17152
  v_mfma_f32_16x16x32_bf16 v[12:15], a[224:227], v[16:19], v[12:15]
  s_waitcnt lgkmcnt(5)
  v_mfma_f32_16x16x32_bf16 v[8:11], a[164:167], v[20:23], v[8:11]
  ds_read_b128 v[16:19], v80 offset:17152
  v_mfma_f32_16x16x32_bf16 v[12:15], a[228:231], v[20:23], v[12:15]
  s_waitcnt lgkmcnt(4)
  v_mfma_f32_16x16x32_bf16 v[8:11], a[168:171], v[24:27], v[8:11]
  ds_read_b128 v[20:23], v81 offset:17152
  v_mfma_f32_16x16x32_bf16 v[12:15], a[232:235], v[24:27], v[12:15]
  s_waitcnt lgkmcnt(4)
  v_mfma_f32_16x16x32_bf16 v[8:11], a[172:175], v[28:31], v[8:11]
  ds_read_b128 v[24:27], v78 offset:0
  v_mfma_f32_16x16x32_bf16 v[12:15], a[236:239], v[28:31], v[12:15]
  s_waitcnt lgkmcnt(4)
  v_mfma_f32_16x16x32_bf16 v[8:11], a[176:179], v[32:35], v[8:11]
  ds_read_b128 v[28:31], v79 offset:0
  v_mfma_f32_16x16x32_bf16 v[12:15], a[240:243], v[32:35], v[12:15]
  s_waitcnt lgkmcnt(4)
  v_mfma_f32_16x16x32_bf16 v[8:11], a[180:183], v[36:39], v[8:11]
  ds_read_b128 v[32:35], v80 offset:0
  v_mfma_f32_16x16x32_bf16 v[12:15], a[244:247], v[36:39], v[12:15]
  s_waitcnt lgkmcnt(4)
  v_mfma_f32_16x16x32_bf16 v[8:11], a[184:187], v[16:19], v[8:11]
  ds_read_b128 v[36:39], v81 offset:0
  v_mfma_f32_16x16x32_bf16 v[12:15], a[248:251], v[16:19], v[12:15]
  s_waitcnt lgkmcnt(4)
  v_mfma_f32_16x16x32_bf16 v[8:11], a[188:191], v[20:23], v[8:11]
  ds_read_b128 v[16:19], v78 offset:256
  v_mfma_f32_16x16x32_bf16 v[12:15], a[252:255], v[20:23], v[12:15]
.Lqa_cont0_%=:
  s_waitcnt lgkmcnt(4)
  v_mfma_f32_16x16x32_bf16 v[0:3], v[24:27], v[128:131], 0
  ds_read_b128 v[20:23], v79 offset:256
  s_nop 0
  v_mfma_f32_16x16x32_bf16 v[4:7], v[24:27], v[192:195], 0
  s_nop 0
  s_nop 0
  s_waitcnt lgkmcnt(4)
  v_mfma_f32_16x16x32_bf16 v[0:3], v[28:31], v[132:135], v[0:3]
  ds_read_b128 v[24:27], v80 offset:256
  s_nop 0
  v_mfma_f32_16x16x32_bf16 v[4:7], v[28:31], v[196:199], v[4:7]
  v_cvt_pk_bf16_f32 v44, v8, v9
  v_cvt_pk_bf16_f32 v45, v10, v11
  s_waitcnt lgkmcnt(4)
  v_mfma_f32_16x16x32_bf16 v[0:3], v[32:35], v[136:139], v[0:3]
  ds_read_b128 v[28:31], v81 offset:256
  v_cvt_pk_bf16_f32 v46, v12, v13
  v_mfma_f32_16x16x32_bf16 v[4:7], v[32:35], v[200:203], v[4:7]
  v_cvt_pk_bf16_f32 v47, v14, v15
  buffer_store_dwordx4 v[44:47], v82, s[20:23], s52 offen offset:1024 sc1
  s_waitcnt lgkmcnt(4)
  v_mfma_f32_16x16x32_bf16 v[0:3], v[36:39], v[140:143], v[0:3]
  ds_read_b128 v[32:35], v78 offset:512
  ds_write_b128 v89, v[44:47] offset:12288
  v_mfma_f32_16x16x32_bf16 v[4:7], v[36:39], v[204:207], v[4:7]
  s_waitcnt lgkmcnt(5)
  v_mfma_f32_16x16x32_bf16 v[0:3], v[16:19], v[144:147], v[0:3]
  ds_read_b128 v[36:39], v79 offset:512
  s_waitcnt vmcnt(3)
  v_mfma_f32_16x16x32_bf16 v[4:7], v[16:19], v[208:211], v[4:7]
  v_cmp_gt_u32 vcc, s48, v84
  v_cmp_gt_u32 s[62:63], s42, v84
  s_waitcnt lgkmcnt(5)
  v_mfma_f32_16x16x32_bf16 v[0:3], v[20:23], v[148:151], v[0:3]
  ds_read_b128 v[16:19], v80 offset:512
  s_cmp_eq_u32 vcc_lo, 0
  v_mfma_f32_16x16x32_bf16 v[4:7], v[20:23], v[212:215], v[4:7]
  s_cselect_b32 s47, 1, 0
  s_and_b32 s56, s63, 15
  s_waitcnt lgkmcnt(5)
  v_mfma_f32_16x16x32_bf16 v[0:3], v[24:27], v[152:155], v[0:3]
  ds_read_b128 v[20:23], v81 offset:512
  s_cselect_b32 s49, -1, s38
  v_mfma_f32_16x16x32_bf16 v[4:7], v[24:27], v[216:219], v[4:7]
  s_mul_i32 s56, s49, 0x40400
  s_and_b32 s56, s56, 0xfffc2000
  s_waitcnt lgkmcnt(5)
  v_mfma_f32_16x16x32_bf16 v[0:3], v[28:31], v[156:159], v[0:3]
  ds_read_b128 v[24:27], v78 offset:768
  s_xor_b32 s61, s56, s32
  v_mfma_f32_16x16x32_bf16 v[4:7], v[28:31], v[220:223], v[4:7]
  s_cmp_lt_i32 s49, 0
  s_waitcnt lgkmcnt(5)
  v_mfma_f32_16x16x32_bf16 v[0:3], v[32:35], v[160:163], v[0:3]
  ds_read_b128 v[28:31], v79 offset:768
  v_mfma_f32_16x16x32_bf16 v[4:7], v[32:35], v[224:227], v[4:7]
  s_cbranch_scc0 .Lqa_job1_%=
  s_waitcnt lgkmcnt(4)
  v_mfma_f32_16x16x32_bf16 v[0:3], v[36:39], v[164:167], v[0:3]
  ds_read_b128 v[32:35], v80 offset:768
  v_mfma_f32_16x16x32_bf16 v[4:7], v[36:39], v[228:231], v[4:7]
  s_cmp_ge_u32 s45, 64
  s_cselect_b32 s57, s1, s30
  s_waitcnt lgkmcnt(4)
  v_mfma_f32_16x16x32_bf16 v[0:3], v[16:19], v[168:171], v[0:3]
  ds_read_b128 v[36:39], v81 offset:768
  s_lshl_b32 s56, s45, 16
  v_mfma_f32_16x16x32_bf16 v[4:7], v[16:19], v[232:235], v[4:7]
  s_add_u32 s56, s56, s57
  s_lshr_b32 s57, s56, 8
  s_waitcnt lgkmcnt(4)
  v_mfma_f32_16x16x32_bf16 v[0:3], v[20:23], v[172:175], v[0:3]
  ds_read_b128 v[16:19], v78 offset:16384
  s_and_b32 s57, s57, 0x2000
  v_mfma_f32_16x16x32_bf16 v[4:7], v[20:23], v[236:239], v[4:7]
  s_xor_b32 s56, s56, s57
  s_or_b32 s46, s46, s47
  s_waitcnt lgkmcnt(4)
  v_mfma_f32_16x16x32_bf16 v[0:3], v[24:27], v[176:179], v[0:3]
  ds_read_b128 v[20:23], v79 offset:16384
  s_cmp_lg_u32 s47, 0
  v_mfma_f32_16x16x32_bf16 v[4:7], v[24:27], v[240:243], v[4:7]
  s_cselect_b32 s50, s56, 0xf0000000
  s_cselect_b32 m0, s51, 0x30000
  s_waitcnt lgkmcnt(4)
  v_mfma_f32_16x16x32_bf16 v[0:3], v[28:31], v[180:183], v[0:3]
  ds_read_b128 v[24:27], v80 offset:16384
  s_cselect_b32 s48, -1, s48
  v_mfma_f32_16x16x32_bf16 v[4:7], v[28:31], v[244:247], v[4:7]
  s_mov_b32 s47, 0
  s_waitcnt lgkmcnt(4)
  v_mfma_f32_16x16x32_bf16 v[0:3], v[32:35], v[184:187], v[0:3]
  ds_read_b128 v[28:31], v81 offset:16384
  v_mfma_f32_16x16x32_bf16 v[4:7], v[32:35], v[248:251], v[4:7]
  s_waitcnt lgkmcnt(4)
  v_mfma_f32_16x16x32_bf16 v[0:3], v[36:39], v[188:191], v[0:3]
  ds_read_b128 v[32:35], v78 offset:16640
  v_mfma_f32_16x16x32_bf16 v[4:7], v[36:39], v[252:255], v[4:7]
  s_waitcnt lgkmcnt(4)
  v_mfma_f32_16x16x32_bf16 v[8:11], v[16:19], v[128:131], 0
  ds_read_b128 v[36:39], v79 offset:16640
  v_mfma_f32_16x16x32_bf16 v[12:15], v[16:19], v[192:195], 0
  s_waitcnt lgkmcnt(4)
  v_mfma_f32_16x16x32_bf16 v[8:11], v[20:23], v[132:135], v[8:11]
  ds_read_b128 v[16:19], v80 offset:16640
  v_mfma_f32_16x16x32_bf16 v[12:15], v[20:23], v[196:199], v[12:15]
  s_waitcnt lgkmcnt(4)
  v_mfma_f32_16x16x32_bf16 v[8:11], v[24:27], v[136:139], v[8:11]
  ds_read_b128 v[20:23], v81 offset:16640
  v_mfma_f32_16x16x32_bf16 v[12:15], v[24:27], v[200:203], v[12:15]
  buffer_load_dword v84, v83, s[20:23], 0 offen offset:0 sc1
  s_waitcnt lgkmcnt(4)
  v_mfma_f32_16x16x32_bf16 v[8:11], v[28:31], v[140:143], v[8:11]
  ds_read_b128 v[24:27], v78 offset:16896
  v_cvt_pk_bf16_f32 v40, v0, v1
  v_mfma_f32_16x16x32_bf16 v[12:15], v[28:31], v[204:207], v[12:15]
  v_cvt_pk_bf16_f32 v41, v2, v3
  v_cvt_pk_bf16_f32 v44, v4, v5
  s_waitcnt lgkmcnt(4)
  v_mfma_f32_16x16x32_bf16 v[8:11], v[32:35], v[144:147], v[8:11]
  ds_read_b128 v[28:31], v79 offset:16896
  v_cvt_pk_bf16_f32 v45, v6, v7
  v_mfma_f32_16x16x32_bf16 v[12:15], v[32:35], v[208:211], v[12:15]
  s_waitcnt lgkmcnt(4)
  v_mfma_f32_16x16x32_bf16 v[8:11], v[36:39], v[148:151], v[8:11]
  ds_read_b128 v[32:35], v80 offset:16896
  v_mfma_f32_16x16x32_bf16 v[12:15], v[36:39], v[212:215], v[12:15]
  s_waitcnt lgkmcnt(4)
  v_mfma_f32_16x16x32_bf16 v[8:11], v[16:19], v[152:155], v[8:11]
  ds_read_b128 v[36:39], v81 offset:16896
  v_mfma_f32_16x16x32_bf16 v[12:15], v[16:19], v[216:219], v[12:15]
  s_waitcnt vmcnt(1)
  v_mov_b32 v88, s54
  s_waitcnt lgkmcnt(4)
  v_mfma_f32_16x16x32_bf16 v[8:11], v[20:23], v[156:159], v[8:11]
  ds_read_b128 v[16:19], v78 offset:17152
  buffer_store_dword v88, v87, s[20:23], s53 offen offset:0 sc1
  v_mfma_f32_16x16x32_bf16 v[12:15], v[20:23], v[220:223], v[12:15]
  s_mov_b32 s53, 0xf0000000
  s_waitcnt lgkmcnt(4)
  v_mfma_f32_16x16x32_bf16 v[8:11], v[24:27], v[160:163], v[8:11]
  ds_read_b128 v[20:23], v79 offset:17152
  v_mfma_f32_16x16x32_bf16 v[12:15], v[24:27], v[224:227], v[12:15]
  buffer_load_dwordx4 v82, s[20:23], s50 offen offset:0 sc1 lds
  s_waitcnt lgkmcnt(4)
  v_mfma_f32_16x16x32_bf16 v[8:11], v[28:31], v[164:167], v[8:11]
  ds_read_b128 v[24:27], v80 offset:17152
  v_mfma_f32_16x16x32_bf16 v[12:15], v[28:31], v[228:231], v[12:15]
  s_waitcnt lgkmcnt(4)
  v_mfma_f32_16x16x32_bf16 v[8:11], v[32:35], v[168:171], v[8:11]
  ds_read_b128 v[28:31], v81 offset:17152
  v_mfma_f32_16x16x32_bf16 v[12:15], v[32:35], v[232:235], v[12:15]
  buffer_load_dwordx4 v82, s[20:23], s50 offen offset:1024 sc1 lds
  s_waitcnt lgkmcnt(4)
  v_mfma_f32_16x16x32_bf16 v[8:11], v[36:39], v[172:175], v[8:11]
  ds_read_b128 v[32:35], v78 offset:0
  v_mfma_f32_16x16x32_bf16 v[12:15], v[36:39], v[236:239], v[12:15]
  s_waitcnt lgkmcnt(4)
  v_mfma_f32_16x16x32_bf16 v[8:11], v[16:19], v[176:179], v[8:11]
  ds_read_b128 v[36:39], v79 offset:0
  v_mfma_f32_16x16x32_bf16 v[12:15], v[16:19], v[240:243], v[12:15]
  buffer_load_dwordx4 v82, s[20:23], s50 offen offset:2048 sc1 lds
  s_waitcnt lgkmcnt(4)
  v_mfma_f32_16x16x32_bf16 v[8:11], v[20:23], v[180:183], v[8:11]
  ds_read_b128 v[16:19], v80 offset:0
  v_mfma_f32_16x16x32_bf16 v[12:15], v[20:23], v[244:247], v[12:15]
  s_waitcnt lgkmcnt(4)
  v_mfma_f32_16x16x32_bf16 v[8:11], v[24:27], v[184:187], v[8:11]
  ds_read_b128 v[20:23], v81 offset:0
  v_mfma_f32_16x16x32_bf16 v[12:15], v[24:27], v[248:251], v[12:15]
  buffer_load_dwordx4 v82, s[20:23], s50 offen offset:3072 sc1 lds
  s_add_u32 m0, m0, 4096
  s_add_u32 s50, s50, 4096
  s_waitcnt lgkmcnt(4)
  v_mfma_f32_16x16x32_bf16 v[8:11], v[28:31], v[188:191], v[8:11]
  ds_read_b128 v[24:27], v78 offset:256
  v_mfma_f32_16x16x32_bf16 v[12:15], v[28:31], v[252:255], v[12:15]
.Lqa_cont1_%=:
  s_waitcnt lgkmcnt(4)
  v_mfma_f32_16x16x32_bf16 v[0:3], a[0:3], v[32:35], 0
  ds_read_b128 v[28:31], v79 offset:256
  s_nop 0
  v_mfma_f32_16x16x32_bf16 v[4:7], a[64:67], v[32:35], 0
  s_nop 0
  s_nop 0
  s_waitcnt lgkmcnt(4)
  v_mfma_f32_16x16x32_bf16 v[0:3], a[4:7], v[36:39], v[0:3]
  ds_read_b128 v[32:35], v80 offset:256
  s_nop 0
  v_mfma_f32_16x16x32_bf16 v[4:7], a[68:71], v[36:39], v[4:7]
  v_cvt_pk_bf16_f32 v42, v8, v9
  v_cvt_pk_bf16_f32 v43, v10, v11
  s_waitcnt lgkmcnt(4)
  v_mfma_f32_16x16x32_bf16 v[0:3], a[8:11], v[16:19], v[0:3]
  ds_read_b128 v[36:39], v81 offset:256
  v_cvt_pk_bf16_f32 v46, v12, v13
  v_mfma_f32_16x16x32_bf16 v[4:7], a[72:75], v[16:19], v[4:7]
  v_cvt_pk_bf16_f32 v47, v14, v15
  buffer_store_dwordx4 v[40:43], v82, s[20:23], s52 offen offset:2048 sc1
  s_waitcnt lgkmcnt(4)
  v_mfma_f32_16x16x32_bf16 v[0:3], a[12:15], v[20:23], v[0:3]
  ds_read_b128 v[16:19], v78 offset:512
  v_mfma_f32_16x16x32_bf16 v[4:7], a[76:79], v[20:23], v[4:7]
  ds_write_b128 v89, v[40:43] offset:16384
  buffer_store_dwordx4 v[44:47], v82, s[20:23], s52 offen offset:3072 sc1
  s_waitcnt lgkmcnt(5)
  v_mfma_f32_16x16x32_bf16 v[0:3], a[16:19], v[24:27], v[0:3]
  ds_read_b128 v[20:23], v79 offset:512
  v_mfma_f32_16x16x32_bf16 v[4:7], a[80:83], v[24:27], v[4:7]
  s_waitcnt vmcnt(7)
  v_cmp_gt_u32 vcc, s48, v84
  v_cmp_gt_u32 s[62:63], s42, v84
  s_waitcnt lgkmcnt(5)
  v_mfma_f32_16x16x32_bf16 v[0:3], a[20:23], v[28:31], v[0:3]
  ds_read_b128 v[24:27], v80 offset:512
  v_cmp_gt_u32 s[64:65], s43, v84
  s_cmp_eq_u32 vcc_lo, 0
  v_mfma_f32_16x16x32_bf16 v[4:7], a[84:87], v[28:31], v[4:7]
  s_cselect_b32 s47, 1, 0
  s_and_b32 s56, s65, 240
  s_cselect_b32 s49, -1, s39
  s_waitcnt lgkmcnt(5)
  v_mfma_f32_16x16x32_bf16 v[0:3], a[24:27], v[32:35], v[0:3]
  ds_read_b128 v[28:31], v81 offset:512
  ds_write_b128 v89, v[44:47] offset:20480
  s_and_b32 s56, s63, 15
  v_mfma_f32_16x16x32_bf16 v[4:7], a[88:91], v[32:35], v[4:7]
  s_cselect_b32 s49, s49, s38
  s_mul_i32 s56, s49, 0x40400
  s_and_b32 s56, s56, 0xfffc2000
  s_waitcnt lgkmcnt(6)
  v_mfma_f32_16x16x32_bf16 v[0:3], a[28:31], v[36:39], v[0:3]
  ds_read_b128 v[32:35], v78 offset:768
  s_xor_b32 s61, s56, s32
  s_cmp_lt_i32 s49, 0
  v_mfma_f32_16x16x32_bf16 v[4:7], a[92:95], v[36:39], v[4:7]
  s_waitcnt lgkmcnt(6)
  v_mfma_f32_16x16x32_bf16 v[0:3], a[32:35], v[16:19], v[0:3]
  ds_read_b128 v[36:39], v79 offset:768
  v_mfma_f32_16x16x32_bf16 v[4:7], a[96:99], v[16:19], v[4:7]
  s_cbranch_scc0 .Lqa_job2_%=
  s_waitcnt lgkmcnt(5)
  v_mfma_f32_16x16x32_bf16 v[0:3], a[36:39], v[20:23], v[0:3]
  ds_read_b128 v[16:19], v80 offset:768
  buffer_load_dwordx4 v82, s[20:23], s50 offen offset:0 sc1 lds
  v_mfma_f32_16x16x32_bf16 v[4:7], a[100:103], v[20:23], v[4:7]
  s_and_b32 s56, s44, 63
  s_add_u32 s56, s56, 1
  s_waitcnt lgkmcnt(5)
  v_mfma_f32_16x16x32_bf16 v[0:3], a[40:43], v[24:27], v[0:3]
  ds_read_b128 v[20:23], v81 offset:768
  v_mov_b32 v88, s56
  v_mfma_f32_16x16x32_bf16 v[4:7], a[104:107], v[24:27], v[4:7]
  s_cmp_ge_u32 s44, 64
  s_cselect_b32 s52, s98, s34
  s_waitcnt lgkmcnt(5)
  v_mfma_f32_16x16x32_bf16 v[0:3], a[44:47], v[28:31], v[0:3]
  ds_read_b128 v[24:27], v78 offset:16384
  buffer_load_dwordx4 v82, s[20:23], s50 offen offset:1024 sc1 lds
  v_mfma_f32_16x16x32_bf16 v[4:7], a[108:111], v[28:31], v[4:7]
  s_cselect_b32 s56, 0, 3
  s_cselect_b32 s57, 3, 0
  s_waitcnt lgkmcnt(4)
  v_mfma_f32_16x16x32_bf16 v[0:3], a[48:51], v[32:35], v[0:3]
  ds_read_b128 v[28:31], v79 offset:16384
  s_add_u32 s40, s40, s56
  v_mfma_f32_16x16x32_bf16 v[4:7], a[112:115], v[32:35], v[4:7]
  s_add_u32 s41, s41, s57
  s_sub_u32 s42, s38, -1
  s_waitcnt lgkmcnt(4)
  v_mfma_f32_16x16x32_bf16 v[0:3], a[52:55], v[36:39], v[0:3]
  ds_read_b128 v[32:35], v80 offset:16384
  buffer_load_dwordx4 v82, s[20:23], s50 offen offset:2048 sc1 lds
  v_mfma_f32_16x16x32_bf16 v[4:7], a[116:119], v[36:39], v[4:7]
  s_cmp_lt_u32 s38, s40
  s_cselect_b32 s42, s42, -1
  s_waitcnt lgkmcnt(4)
  v_mfma_f32_16x16x32_bf16 v[0:3], a[56:59], v[16:19], v[0:3]
  ds_read_b128 v[36:39], v81 offset:16384
  s_sub_u32 s43, s39, 63
  v_mfma_f32_16x16x32_bf16 v[4:7], a[120:123], v[16:19], v[4:7]
  s_cmp_lt_u32 s39, s41
  s_cselect_b32 s43, s43, -1
  s_waitcnt lgkmcnt(4)
  v_mfma_f32_16x16x32_bf16 v[0:3], a[60:63], v[20:23], v[0:3]
  ds_read_b128 v[16:19], v78 offset:16640
  buffer_load_dwordx4 v82, s[20:23], s50 offen offset:3072 sc1 lds
  v_mfma_f32_16x16x32_bf16 v[4:7], a[124:127], v[20:23], v[4:7]
  s_waitcnt lgkmcnt(4)
  v_mfma_f32_16x16x32_bf16 v[8:11], a[0:3], v[24:27], 0
  ds_read_b128 v[20:23], v79 offset:16640
  v_mfma_f32_16x16x32_bf16 v[12:15], a[64:67], v[24:27], 0
  s_waitcnt lgkmcnt(4)
  v_mfma_f32_16x16x32_bf16 v[8:11], a[4:7], v[28:31], v[8:11]
  ds_read_b128 v[24:27], v80 offset:16640
  v_mfma_f32_16x16x32_bf16 v[12:15], a[68:71], v[28:31], v[12:15]
  s_waitcnt lgkmcnt(4)
  v_mfma_f32_16x16x32_bf16 v[8:11], a[8:11], v[32:35], v[8:11]
  ds_read_b128 v[28:31], v81 offset:16640
  v_mfma_f32_16x16x32_bf16 v[12:15], a[72:75], v[32:35], v[12:15]
  buffer_load_dword v84, v83, s[20:23], 0 offen offset:0 sc1
  s_waitcnt lgkmcnt(4)
  v_mfma_f32_16x16x32_bf16 v[8:11], a[12:15], v[36:39], v[8:11]
  ds_read_b128 v[32:35], v78 offset:16896
  v_cvt_pk_bf16_f32 v40, v0, v1
  v_mfma_f32_16x16x32_bf16 v[12:15], a[76:79], v[36:39], v[12:15]
  v_cvt_pk_bf16_f32 v41, v2, v3
  v_cvt_pk_bf16_f32 v42, v4, v5
  s_waitcnt lgkmcnt(4)
  v_mfma_f32_16x16x32_bf16 v[8:11], a[16:19], v[16:19], v[8:11]
  ds_read_b128 v[36:39], v79 offset:16896
  v_cvt_pk_bf16_f32 v43, v6, v7
  v_mfma_f32_16x16x32_bf16 v[12:15], a[80:83], v[16:19], v[12:15]
  ds_write_b128 v89, v[40:43] offset:0
  s_waitcnt lgkmcnt(5)
  v_mfma_f32_16x16x32_bf16 v[8:11], a[20:23], v[20:23], v[8:11]
  ds_read_b128 v[16:19], v80 offset:16896
  v_mfma_f32_16x16x32_bf16 v[12:15], a[84:87], v[20:23], v[12:15]
  s_waitcnt lgkmcnt(5)
  v_mfma_f32_16x16x32_bf16 v[8:11], a[24:27], v[24:27], v[8:11]
  ds_read_b128 v[20:23], v81 offset:16896
  v_mfma_f32_16x16x32_bf16 v[12:15], a[88:91], v[24:27], v[12:15]
  s_waitcnt vmcnt(5)
  buffer_store_dword v88, v87, s[20:23], s52 offen offset:0 sc1
  s_waitcnt lgkmcnt(5)
  v_mfma_f32_16x16x32_bf16 v[8:11], a[28:31], v[28:31], v[8:11]
  ds_read_b128 v[24:27], v78 offset:17152
  v_mov_b32 v88, s54
  v_mfma_f32_16x16x32_bf16 v[12:15], a[92:95], v[28:31], v[12:15]
  buffer_store_dword v88, v87, s[20:23], s53 offen offset:0 sc1
  s_mov_b32 s53, 0xf0000000
  s_waitcnt lgkmcnt(5)
  v_mfma_f32_16x16x32_bf16 v[8:11], a[32:35], v[32:35], v[8:11]
  ds_read_b128 v[28:31], v79 offset:17152
  v_mfma_f32_16x16x32_bf16 v[12:15], a[96:99], v[32:35], v[12:15]
  s_waitcnt lgkmcnt(5)
  v_mfma_f32_16x16x32_bf16 v[8:11], a[36:39], v[36:39], v[8:11]
  ds_read_b128 v[32:35], v80 offset:17152
  v_mfma_f32_16x16x32_bf16 v[12:15], a[100:103], v[36:39], v[12:15]
  s_waitcnt lgkmcnt(4)
  v_mfma_f32_16x16x32_bf16 v[8:11], a[40:43], v[16:19], v[8:11]
  ds_read_b128 v[36:39], v81 offset:17152
  v_mfma_f32_16x16x32_bf16 v[12:15], a[104:107], v[16:19], v[12:15]
  s_waitcnt lgkmcnt(4)
  v_mfma_f32_16x16x32_bf16 v[8:11], a[44:47], v[20:23], v[8:11]
  v_mfma_f32_16x16x32_bf16 v[12:15], a[108:111], v[20:23], v[12:15]
  s_waitcnt lgkmcnt(3)
  v_mfma_f32_16x16x32_bf16 v[8:11], a[48:51], v[24:27], v[8:11]
  v_mfma_f32_16x16x32_bf16 v[12:15], a[112:115], v[24:27], v[12:15]
  s_waitcnt lgkmcnt(2)
  v_mfma_f32_16x16x32_bf16 v[8:11], a[52:55], v[28:31], v[8:11]
  v_mfma_f32_16x16x32_bf16 v[12:15], a[116:119], v[28:31], v[12:15]
  s_waitcnt lgkmcnt(1)
  v_mfma_f32_16x16x32_bf16 v[8:11], a[56:59], v[32:35], v[8:11]
  v_mfma_f32_16x16x32_bf16 v[12:15], a[120:123], v[32:35], v[12:15]
  s_waitcnt lgkmcnt(0)
  v_mfma_f32_16x16x32_bf16 v[8:11], a[60:63], v[36:39], v[8:11]
  v_mfma_f32_16x16x32_bf16 v[12:15], a[124:127], v[36:39], v[12:15]
  s_nop 6
  v_cvt_pk_bf16_f32 v44, v8, v9
  v_cvt_pk_bf16_f32 v45, v10, v11
  v_cvt_pk_bf16_f32 v46, v12, v13
  v_cvt_pk_bf16_f32 v47, v14, v15
  ds_write_b128 v89, v[44:47] offset:4096
  s_waitcnt lgkmcnt(0)
  s_barrier
  .if DBGT
  s_memrealtime s[72:73]
  s_lshl_b32 s74, s44, 2
  s_or_b32 s74, s74, 2
  s_waitcnt lgkmcnt(0)
  s_lshl_b32 s72, s72, 10
  s_or_b32 s72, s72, s74
  v_mov_b32 v88, s72
  buffer_store_dword v88, v87, s[20:23], s70 offen
  s_add_u32 s70, s70, 4
  .endif
.Lqa_cont2_%=:
  s_cmp_eq_u32 s46, 0
  s_cbranch_scc1 .Lqa_idle2_%=
  s_cmp_ge_u32 s45, 64
  s_cselect_b32 s56, s39, s38
  s_add_u32 s56, s56, 6
  s_cmp_gt_u32 s56, s45
  s_cbranch_scc0 .Lqa_idle2_%=
  s_waitcnt vmcnt(2)
  s_branch .Lqa_top_%=
.Lqa_job0_%=:
  s_waitcnt lgkmcnt(4)
  v_mfma_f32_16x16x32_bf16 v[0:3], a[164:167], v[28:31], v[0:3]
  ds_read_b128 v[24:27], v80 offset:768
  buffer_load_dwordx4 v[48:51], v85, s[20:23], s61 offen offset:0 sc1
  v_mfma_f32_16x16x32_bf16 v[4:7], a[228:231], v[28:31], v[4:7]
  s_and_b32 s56, s44, 1
  s_cmp_ge_u32 s44, 64
  s_waitcnt lgkmcnt(4)
  v_mfma_f32_16x16x32_bf16 v[0:3], a[168:171], v[32:35], v[0:3]
  ds_read_b128 v[28:31], v81 offset:768
  s_addc_u32 s56, s56, s56
  v_mfma_f32_16x16x32_bf16 v[4:7], a[232:235], v[32:35], v[4:7]
  s_mul_i32 s56, s56, 24576
  s_add_u32 s56, s56, 65536
  s_waitcnt lgkmcnt(4)
  v_mfma_f32_16x16x32_bf16 v[0:3], a[172:175], v[36:39], v[0:3]
  ds_read_b128 v[32:35], v78 offset:16384
  buffer_load_dwordx4 v[52:55], v85, s[20:23], s61 offen offset:512 sc1
  v_mfma_f32_16x16x32_bf16 v[4:7], a[236:239], v[36:39], v[4:7]
  s_add_u32 s56, s56, s68
  v_add_u32 v89, s56, v82
  s_waitcnt lgkmcnt(4)
  v_mfma_f32_16x16x32_bf16 v[0:3], a[176:179], v[16:19], v[0:3]
  ds_read_b128 v[36:39], v79 offset:16384
  s_and_b32 s52, s44, 63
  v_mfma_f32_16x16x32_bf16 v[4:7], a[240:243], v[16:19], v[4:7]
  s_lshl_b32 s52, s52, 20
  s_lshr_b32 s56, s52, 8
  s_waitcnt lgkmcnt(4)
  v_mfma_f32_16x16x32_bf16 v[0:3], a[180:183], v[20:23], v[0:3]
  ds_read_b128 v[16:19], v80 offset:16384
  buffer_load_dwordx4 v[56:59], v85, s[20:23], s61 offen offset:1024 sc1
  v_mfma_f32_16x16x32_bf16 v[4:7], a[244:247], v[20:23], v[4:7]
  s_and_b32 s56, s56, 0x2000
  s_add_u32 s52, s52, s31
  s_waitcnt lgkmcnt(4)
  v_mfma_f32_16x16x32_bf16 v[0:3], a[184:187], v[24:27], v[0:3]
  ds_read_b128 v[20:23], v81 offset:16384
  s_xor_b32 s52, s52, s56
  v_mfma_f32_16x16x32_bf16 v[4:7], a[248:251], v[24:27], v[4:7]
  s_waitcnt lgkmcnt(4)
  v_mfma_f32_16x16x32_bf16 v[0:3], a[188:191], v[28:31], v[0:3]
  ds_read_b128 v[24:27], v78 offset:16640
  buffer_load_dwordx4 v[60:63], v85, s[20:23], s61 offen offset:1536 sc1
  v_mfma_f32_16x16x32_bf16 v[4:7], a[252:255], v[28:31], v[4:7]
  s_waitcnt lgkmcnt(4)
  v_mfma_f32_16x16x32_bf16 v[8:11], a[128:131], v[32:35], 0
  ds_read_b128 v[28:31], v79 offset:16640
  v_mfma_f32_16x16x32_bf16 v[12:15], a[192:195], v[32:35], 0
  buffer_load_dwordx2 v[64:65], v86, s[20:23], s61 offen offset:0 sc1
  s_waitcnt lgkmcnt(4)
  v_mfma_f32_16x16x32_bf16 v[8:11], a[132:135], v[36:39], v[8:11]
  ds_read_b128 v[32:35], v80 offset:16640
  v_mfma_f32_16x16x32_bf16 v[12:15], a[196:199], v[36:39], v[12:15]
  buffer_load_dwordx2 v[66:67], v86, s[20:23], s61 offen offset:512 sc1
  s_waitcnt lgkmcnt(4)
  v_mfma_f32_16x16x32_bf16 v[8:11], a[136:139], v[16:19], v[8:11]
  ds_read_b128 v[36:39], v81 offset:16640
  v_mfma_f32_16x16x32_bf16 v[12:15], a[200:203], v[16:19], v[12:15]
  buffer_load_dword v84, v83, s[20:23], 0 offen offset:0 sc1
  s_waitcnt lgkmcnt(4)
  v_mfma_f32_16x16x32_bf16 v[8:11], a[140:143], v[20:23], v[8:11]
  ds_read_b128 v[16:19], v78 offset:16896
  v_cvt_pk_bf16_f32 v40, v0, v1
  v_mfma_f32_16x16x32_bf16 v[12:15], a[204:207], v[20:23], v[12:15]
  v_cvt_pk_bf16_f32 v41, v2, v3
  v_cvt_pk_bf16_f32 v42, v4, v5
  s_waitcnt lgkmcnt(4)
  v_mfma_f32_16x16x32_bf16 v[8:11], a[144:147], v[24:27], v[8:11]
  ds_read_b128 v[20:23], v79 offset:16896
  v_cvt_pk_bf16_f32 v43, v6, v7
  v_mfma_f32_16x16x32_bf16 v[12:15], a[208:211], v[24:27], v[12:15]
  buffer_store_dwordx4 v[40:43], v82, s[20:23], s52 offen offset:0 sc1
  ds_write_b128 v89, v[40:43] offset:8192
  s_waitcnt lgkmcnt(5)
  v_mfma_f32_16x16x32_bf16 v[8:11], a[148:151], v[28:31], v[8:11]
  ds_read_b128 v[24:27], v80 offset:16896
  v_mfma_f32_16x16x32_bf16 v[12:15], a[212:215], v[28:31], v[12:15]
  s_waitcnt lgkmcnt(5)
  v_mfma_f32_16x16x32_bf16 v[8:11], a[152:155], v[32:35], v[8:11]
  ds_read_b128 v[28:31], v81 offset:16896
  v_mfma_f32_16x16x32_bf16 v[12:15], a[216:219], v[32:35], v[12:15]
  s_waitcnt vmcnt(8)
  v_mov_b32 v88, s54
  s_waitcnt lgkmcnt(5)
  v_mfma_f32_16x16x32_bf16 v[8:11], a[156:159], v[36:39], v[8:11]
  ds_read_b128 v[32:35], v78 offset:17152
  buffer_store_dword v88, v87, s[20:23], s53 offen offset:0 sc1
  v_mfma_f32_16x16x32_bf16 v[12:15], a[220:223], v[36:39], v[12:15]
  s_mov_b32 s53, 0xf0000000
  s_waitcnt lgkmcnt(5)
  v_mfma_f32_16x16x32_bf16 v[8:11], a[160:163], v[16:19], v[8:11]
  ds_read_b128 v[36:39], v79 offset:17152
  v_mfma_f32_16x16x32_bf16 v[12:15], a[224:227], v[16:19], v[12:15]
  s_waitcnt lgkmcnt(5)
  v_mfma_f32_16x16x32_bf16 v[8:11], a[164:167], v[20:23], v[8:11]
  ds_read_b128 v[16:19], v80 offset:17152
  v_mfma_f32_16x16x32_bf16 v[12:15], a[228:231], v[20:23], v[12:15]
  s_waitcnt lgkmcnt(4)
  v_mfma_f32_16x16x32_bf16 v[8:11], a[168:171], v[24:27], v[8:11]
  ds_read_b128 v[20:23], v81 offset:17152
  v_mfma_f32_16x16x32_bf16 v[12:15], a[232:235], v[24:27], v[12:15]
  s_waitcnt lgkmcnt(4)
  v_mfma_f32_16x16x32_bf16 v[8:11], a[172:175], v[28:31], v[8:11]
  ds_read_b128 v[24:27], v78 offset:0
  v_mfma_f32_16x16x32_bf16 v[12:15], a[236:239], v[28:31], v[12:15]
  s_waitcnt lgkmcnt(4)
  v_mfma_f32_16x16x32_bf16 v[8:11], a[176:179], v[32:35], v[8:11]
  ds_read_b128 v[28:31], v79 offset:0
  v_mfma_f32_16x16x32_bf16 v[12:15], a[240:243], v[32:35], v[12:15]
  s_waitcnt lgkmcnt(4)
  v_mfma_f32_16x16x32_bf16 v[8:11], a[180:183], v[36:39], v[8:11]
  ds_read_b128 v[32:35], v80 offset:0
  v_mfma_f32_16x16x32_bf16 v[12:15], a[244:247], v[36:39], v[12:15]
  s_waitcnt lgkmcnt(4)
  v_mfma_f32_16x16x32_bf16 v[8:11], a[184:187], v[16:19], v[8:11]
  ds_read_b128 v[36:39], v81 offset:0
  v_mfma_f32_16x16x32_bf16 v[12:15], a[248:251], v[16:19], v[12:15]
  s_waitcnt lgkmcnt(4)
  v_mfma_f32_16x16x32_bf16 v[8:11], a[188:191], v[20:23], v[8:11]
  ds_read_b128 v[16:19], v78 offset:256
  v_mfma_f32_16x16x32_bf16 v[12:15], a[252:255], v[20:23], v[12:15]
  s_waitcnt vmcnt(3)
  s_call_b64 s[66:67], .Lqa_fin_%=
  s_branch .Lqa_cont0_%=
.Lqa_job1_%=:
  s_waitcnt lgkmcnt(4)
  v_mfma_f32_16x16x32_bf16 v[0:3], v[36:39], v[164:167], v[0:3]
  ds_read_b128 v[32:35], v80 offset:768
  buffer_load_dwordx4 v[48:51], v85, s[20:23], s61 offen offset:0 sc1
  v_mfma_f32_16x16x32_bf16 v[4:7], v[36:39], v[228:231], v[4:7]
  s_cmp_ge_u32 s45, 64
  s_cselect_b32 s57, s1, s30
  s_waitcnt lgkmcnt(4)
  v_mfma_f32_16x16x32_bf16 v[0:3], v[16:19], v[168:171], v[0:3]
  ds_read_b128 v[36:39], v81 offset:768
  s_lshl_b32 s56, s45, 16
  v_mfma_f32_16x16x32_bf16 v[4:7], v[16:19], v[232:235], v[4:7]
  s_add_u32 s56, s56, s57
  s_lshr_b32 s57, s56, 8
  s_waitcnt lgkmcnt(4)
  v_mfma_f32_16x16x32_bf16 v[0:3], v[20:23], v[172:175], v[0:3]
  ds_read_b128 v[16:19], v78 offset:16384
  buffer_load_dwordx4 v[52:55], v85, s[20:23], s61 offen offset:512 sc1
  v_mfma_f32_16x16x32_bf16 v[4:7], v[20:23], v[236:239], v[4:7]
  s_and_b32 s57, s57, 0x2000
  s_xor_b32 s56, s56, s57
  s_waitcnt lgkmcnt(4)
  v_mfma_f32_16x16x32_bf16 v[0:3], v[24:27], v[176:179], v[0:3]
  ds_read_b128 v[20:23], v79 offset:16384
  s_or_b32 s46, s46, s47
  v_mfma_f32_16x16x32_bf16 v[4:7], v[24:27], v[240:243], v[4:7]
  s_cmp_lg_u32 s47, 0
  s_cselect_b32 s50, s56, 0xf0000000
  s_waitcnt lgkmcnt(4)
  v_mfma_f32_16x16x32_bf16 v[0:3], v[28:31], v[180:183], v[0:3]
  ds_read_b128 v[24:27], v80 offset:16384
  buffer_load_dwordx4 v[56:59], v85, s[20:23], s61 offen offset:1024 sc1
  v_mfma_f32_16x16x32_bf16 v[4:7], v[28:31], v[244:247], v[4:7]
  s_cselect_b32 m0, s51, 0x30000
  s_cselect_b32 s48, -1, s48
  s_waitcnt lgkmcnt(4)
  v_mfma_f32_16x16x32_bf16 v[0:3], v[32:35], v[184:187], v[0:3]
  ds_read_b128 v[28:31], v81 offset:16384
  s_mov_b32 s47, 0
  v_mfma_f32_16x16x32_bf16 v[4:7], v[32:35], v[248:251], v[4:7]
  s_waitcnt lgkmcnt(4)
  v_mfma_f32_16x16x32_bf16 v[0:3], v[36:39], v[188:191], v[0:3]
  ds_read_b128 v[32:35], v78 offset:16640
  buffer_load_dwordx4 v[60:63], v85, s[20:23], s61 offen offset:1536 sc1
  v_mfma_f32_16x16x32_bf16 v[4:7], v[36:39], v[252:255], v[4:7]
  s_waitcnt lgkmcnt(4)
  v_mfma_f32_16x16x32_bf16 v[8:11], v[16:19], v[128:131], 0
  ds_read_b128 v[36:39], v79 offset:16640
  v_mfma_f32_16x16x32_bf16 v[12:15], v[16:19], v[192:195], 0
  buffer_load_dwordx2 v[64:65], v86, s[20:23], s61 offen offset:0 sc1
  s_waitcnt lgkmcnt(4)
  v_mfma_f32_16x16x32_bf16 v[8:11], v[20:23], v[132:135], v[8:11]
  ds_read_b128 v[16:19], v80 offset:16640
  v_mfma_f32_16x16x32_bf16 v[12:15], v[20:23], v[196:199], v[12:15]
  buffer_load_dwordx2 v[66:67], v86, s[20:23], s61 offen offset:512 sc1
  s_waitcnt lgkmcnt(4)
  v_mfma_f32_16x16x32_bf16 v[8:11], v[24:27], v[136:139], v[8:11]
  ds_read_b128 v[20:23], v81 offset:16640
  v_mfma_f32_16x16x32_bf16 v[12:15], v[24:27], v[200:203], v[12:15]
  buffer_load_dword v84, v83, s[20:23], 0 offen offset:0 sc1
  s_waitcnt lgkmcnt(4)
  v_mfma_f32_16x16x32_bf16 v[8:11], v[28:31], v[140:143], v[8:11]
  ds_read_b128 v[24:27], v78 offset:16896
  v_cvt_pk_bf16_f32 v40, v0, v1
  v_mfma_f32_16x16x32_bf16 v[12:15], v[28:31], v[204:207], v[12:15]
  v_cvt_pk_bf16_f32 v41, v2, v3
  v_cvt_pk_bf16_f32 v44, v4, v5
  s_waitcnt lgkmcnt(4)
  v_mfma_f32_16x16x32_bf16 v[8:11], v[32:35], v[144:147], v[8:11]
  ds_read_b128 v[28:31], v79 offset:16896
  v_cvt_pk_bf16_f32 v45, v6, v7
  v_mfma_f32_16x16x32_bf16 v[12:15], v[32:35], v[208:211], v[12:15]
  s_waitcnt lgkmcnt(4)
  v_mfma_f32_16x16x32_bf16 v[8:11], v[36:39], v[148:151], v[8:11]
  ds_read_b128 v[32:35], v80 offset:16896
  v_mfma_f32_16x16x32_bf16 v[12:15], v[36:39], v[212:215], v[12:15]
  s_waitcnt lgkmcnt(4)
  v_mfma_f32_16x16x32_bf16 v[8:11], v[16:19], v[152:155], v[8:11]
  ds_read_b128 v[36:39], v81 offset:16896
  v_mfma_f32_16x16x32_bf16 v[12:15], v[16:19], v[216:219], v[12:15]
  s_waitcnt vmcnt(7)
  v_mov_b32 v88, s54
  s_waitcnt lgkmcnt(4)
  v_mfma_f32_16x16x32_bf16 v[8:11], v[20:23], v[156:159], v[8:11]
  ds_read_b128 v[16:19], v78 offset:17152
  buffer_store_dword v88, v87, s[20:23], s53 offen offset:0 sc1
  v_mfma_f32_16x16x32_bf16 v[12:15], v[20:23], v[220:223], v[12:15]
  s_mov_b32 s53, 0xf0000000
  s_waitcnt lgkmcnt(4)
  v_mfma_f32_16x16x32_bf16 v[8:11], v[24:27], v[160:163], v[8:11]
  ds_read_b128 v[20:23], v79 offset:17152
  v_mfma_f32_16x16x32_bf16 v[12:15], v[24:27], v[224:227], v[12:15]
  buffer_load_dwordx4 v82, s[20:23], s50 offen offset:0 sc1 lds
  s_waitcnt lgkmcnt(4)
  v_mfma_f32_16x16x32_bf16 v[8:11], v[28:31], v[164:167], v[8:11]
  ds_read_b128 v[24:27], v80 offset:17152
  v_mfma_f32_16x16x32_bf16 v[12:15], v[28:31], v[228:231], v[12:15]
  s_waitcnt lgkmcnt(4)
  v_mfma_f32_16x16x32_bf16 v[8:11], v[32:35], v[168:171], v[8:11]
  ds_read_b128 v[28:31], v81 offset:17152
  v_mfma_f32_16x16x32_bf16 v[12:15], v[32:35], v[232:235], v[12:15]
  buffer_load_dwordx4 v82, s[20:23], s50 offen offset:1024 sc1 lds
  s_waitcnt lgkmcnt(4)
  v_mfma_f32_16x16x32_bf16 v[8:11], v[36:39], v[172:175], v[8:11]
  ds_read_b128 v[32:35], v78 offset:0
  v_mfma_f32_16x16x32_bf16 v[12:15], v[36:39], v[236:239], v[12:15]
  s_waitcnt lgkmcnt(4)
  v_mfma_f32_16x16x32_bf16 v[8:11], v[16:19], v[176:179], v[8:11]
  ds_read_b128 v[36:39], v79 offset:0
  v_mfma_f32_16x16x32_bf16 v[12:15], v[16:19], v[240:243], v[12:15]
  buffer_load_dwordx4 v82, s[20:23], s50 offen offset:2048 sc1 lds
  s_waitcnt lgkmcnt(4)
  v_mfma_f32_16x16x32_bf16 v[8:11], v[20:23], v[180:183], v[8:11]
  ds_read_b128 v[16:19], v80 offset:0
  v_mfma_f32_16x16x32_bf16 v[12:15], v[20:23], v[244:247], v[12:15]
  s_waitcnt lgkmcnt(4)
  v_mfma_f32_16x16x32_bf16 v[8:11], v[24:27], v[184:187], v[8:11]
  ds_read_b128 v[20:23], v81 offset:0
  v_mfma_f32_16x16x32_bf16 v[12:15], v[24:27], v[248:251], v[12:15]
  buffer_load_dwordx4 v82, s[20:23], s50 offen offset:3072 sc1 lds
  s_add_u32 m0, m0, 4096
  s_add_u32 s50, s50, 4096
  s_waitcnt lgkmcnt(4)
  v_mfma_f32_16x16x32_bf16 v[8:11], v[28:31], v[188:191], v[8:11]
  ds_read_b128 v[24:27], v78 offset:256
  v_mfma_f32_16x16x32_bf16 v[12:15], v[28:31], v[252:255], v[12:15]
  s_waitcnt vmcnt(6)
  s_call_b64 s[66:67], .Lqa_fin_%=
  s_branch .Lqa_cont1_%=
.Lqa_job2_%=:
  s_waitcnt lgkmcnt(5)
  v_mfma_f32_16x16x32_bf16 v[0:3], a[36:39], v[20:23], v[0:3]
  ds_read_b128 v[16:19], v80 offset:768
  buffer_load_dwordx4 v[48:51], v85, s[20:23], s61 offen offset:0 sc1
  v_mfma_f32_16x16x32_bf16 v[4:7], a[100:103], v[20:23], v[4:7]
  s_and_b32 s56, s44, 63
  s_add_u32 s56, s56, 1
  s_waitcnt lgkmcnt(5)
  v_mfma_f32_16x16x32_bf16 v[0:3], a[40:43], v[24:27], v[0:3]
  ds_read_b128 v[20:23], v81 offset:768
  v_mov_b32 v88, s56
  v_mfma_f32_16x16x32_bf16 v[4:7], a[104:107], v[24:27], v[4:7]
  s_cmp_ge_u32 s44, 64
  s_cselect_b32 s52, s98, s34
  s_waitcnt lgkmcnt(5)
  v_mfma_f32_16x16x32_bf16 v[0:3], a[44:47], v[28:31], v[0:3]
  ds_read_b128 v[24:27], v78 offset:16384
  buffer_load_dwordx4 v[52:55], v85, s[20:23], s61 offen offset:512 sc1
  v_mfma_f32_16x16x32_bf16 v[4:7], a[108:111], v[28:31], v[4:7]
  s_cselect_b32 s56, 0, 3
  s_cselect_b32 s57, 3, 0
  s_waitcnt lgkmcnt(4)
  v_mfma_f32_16x16x32_bf16 v[0:3], a[48:51], v[32:35], v[0:3]
  ds_read_b128 v[28:31], v79 offset:16384
  s_add_u32 s40, s40, s56
  v_mfma_f32_16x16x32_bf16 v[4:7], a[112:115], v[32:35], v[4:7]
  s_add_u32 s41, s41, s57
  s_sub_u32 s42, s38, -1
  s_waitcnt lgkmcnt(4)
  v_mfma_f32_16x16x32_bf16 v[0:3], a[52:55], v[36:39], v[0:3]
  ds_read_b128 v[32:35], v80 offset:16384
  buffer_load_dwordx4 v[56:59], v85, s[20:23], s61 offen offset:1024 sc1
  v_mfma_f32_16x16x32_bf16 v[4:7], a[116:119], v[36:39], v[4:7]
  s_cmp_lt_u32 s38, s40
  s_cselect_b32 s42, s42, -1
  s_waitcnt lgkmcnt(4)
  v_mfma_f32_16x16x32_bf16 v[0:3], a[56:59], v[16:19], v[0:3]
  ds_read_b128 v[36:39], v81 offset:16384
  s_sub_u32 s43, s39, 63
  v_mfma_f32_16x16x32_bf16 v[4:7], a[120:123], v[16:19], v[4:7]
  s_cmp_lt_u32 s39, s41
  s_cselect_b32 s43, s43, -1
  s_waitcnt lgkmcnt(4)
  v_mfma_f32_16x16x32_bf16 v[0:3], a[60:63], v[20:23], v[0:3]
  ds_read_b128 v[16:19], v78 offset:16640
  buffer_load_dwordx4 v[60:63], v85, s[20:23], s61 offen offset:1536 sc1
  v_mfma_f32_16x16x32_bf16 v[4:7], a[124:127], v[20:23], v[4:7]
  s_waitcnt lgkmcnt(4)
  v_mfma_f32_16x16x32_bf16 v[8:11], a[0:3], v[24:27], 0
  ds_read_b128 v[20:23], v79 offset:16640
  v_mfma_f32_16x16x32_bf16 v[12:15], a[64:67], v[24:27], 0
  buffer_load_dwordx2 v[64:65], v86, s[20:23], s61 offen offset:0 sc1
  s_waitcnt lgkmcnt(4)
  v_mfma_f32_16x16x32_bf16 v[8:11], a[4:7], v[28:31], v[8:11]
  ds_read_b128 v[24:27], v80 offset:16640
  v_mfma_f32_16x16x32_bf16 v[12:15], a[68:71], v[28:31], v[12:15]
  buffer_load_dwordx2 v[66:67], v86, s[20:23], s61 offen offset:512 sc1
  s_waitcnt lgkmcnt(4)
  v_mfma_f32_16x16x32_bf16 v[8:11], a[8:11], v[32:35], v[8:11]
  ds_read_b128 v[28:31], v81 offset:16640
  v_mfma_f32_16x16x32_bf16 v[12:15], a[72:75], v[32:35], v[12:15]
  buffer_load_dword v84, v83, s[20:23], 0 offen offset:0 sc1
  s_waitcnt lgkmcnt(4)
  v_mfma_f32_16x16x32_bf16 v[8:11], a[12:15], v[36:39], v[8:11]
  ds_read_b128 v[32:35], v78 offset:16896
  v_cvt_pk_bf16_f32 v40, v0, v1
  v_mfma_f32_16x16x32_bf16 v[12:15], a[76:79], v[36:39], v[12:15]
  v_cvt_pk_bf16_f32 v41, v2, v3
  v_cvt_pk_bf16_f32 v42, v4, v5
  s_waitcnt lgkmcnt(4)
  v_mfma_f32_16x16x32_bf16 v[8:11], a[16:19], v[16:19], v[8:11]
  ds_read_b128 v[36:39], v79 offset:16896
  v_cvt_pk_bf16_f32 v43, v6, v7
  v_mfma_f32_16x16x32_bf16 v[12:15], a[80:83], v[16:19], v[12:15]
  ds_write_b128 v89, v[40:43] offset:0
  s_waitcnt lgkmcnt(5)
  v_mfma_f32_16x16x32_bf16 v[8:11], a[20:23], v[20:23], v[8:11]
  ds_read_b128 v[16:19], v80 offset:16896
  v_mfma_f32_16x16x32_bf16 v[12:15], a[84:87], v[20:23], v[12:15]
  s_waitcnt lgkmcnt(5)
  v_mfma_f32_16x16x32_bf16 v[8:11], a[24:27], v[24:27], v[8:11]
  ds_read_b128 v[20:23], v81 offset:16896
  v_mfma_f32_16x16x32_bf16 v[12:15], a[88:91], v[24:27], v[12:15]
  s_waitcnt vmcnt(7)
  buffer_store_dword v88, v87, s[20:23], s52 offen offset:0 sc1
  s_waitcnt lgkmcnt(5)
  v_mfma_f32_16x16x32_bf16 v[8:11], a[28:31], v[28:31], v[8:11]
  ds_read_b128 v[24:27], v78 offset:17152
  v_mov_b32 v88, s54
  v_mfma_f32_16x16x32_bf16 v[12:15], a[92:95], v[28:31], v[12:15]
  buffer_store_dword v88, v87, s[20:23], s53 offen offset:0 sc1
  s_mov_b32 s53, 0xf0000000
  s_waitcnt lgkmcnt(5)
  v_mfma_f32_16x16x32_bf16 v[8:11], a[32:35], v[32:35], v[8:11]
  ds_read_b128 v[28:31], v79 offset:17152
  v_mfma_f32_16x16x32_bf16 v[12:15], a[96:99], v[32:35], v[12:15]
  buffer_load_dwordx4 v82, s[20:23], s50 offen offset:0 sc1 lds
  s_waitcnt lgkmcnt(5)
  v_mfma_f32_16x16x32_bf16 v[8:11], a[36:39], v[36:39], v[8:11]
  ds_read_b128 v[32:35], v80 offset:17152
  v_mfma_f32_16x16x32_bf16 v[12:15], a[100:103], v[36:39], v[12:15]
  s_waitcnt lgkmcnt(4)
  v_mfma_f32_16x16x32_bf16 v[8:11], a[40:43], v[16:19], v[8:11]
  ds_read_b128 v[36:39], v81 offset:17152
  v_mfma_f32_16x16x32_bf16 v[12:15], a[104:107], v[16:19], v[12:15]
  buffer_load_dwordx4 v82, s[20:23], s50 offen offset:1024 sc1 lds
  s_waitcnt lgkmcnt(4)
  v_mfma_f32_16x16x32_bf16 v[8:11], a[44:47], v[20:23], v[8:11]
  v_mfma_f32_16x16x32_bf16 v[12:15], a[108:111], v[20:23], v[12:15]
  s_waitcnt lgkmcnt(3)
  v_mfma_f32_16x16x32_bf16 v[8:11], a[48:51], v[24:27], v[8:11]
  v_mfma_f32_16x16x32_bf16 v[12:15], a[112:115], v[24:27], v[12:15]
  buffer_load_dwordx4 v82, s[20:23], s50 offen offset:2048 sc1 lds
  s_waitcnt lgkmcnt(2)
  v_mfma_f32_16x16x32_bf16 v[8:11], a[52:55], v[28:31], v[8:11]
  v_mfma_f32_16x16x32_bf16 v[12:15], a[116:119], v[28:31], v[12:15]
  s_waitcnt lgkmcnt(1)
  v_mfma_f32_16x16x32_bf16 v[8:11], a[56:59], v[32:35], v[8:11]
  v_mfma_f32_16x16x32_bf16 v[12:15], a[120:123], v[32:35], v[12:15]
  buffer_load_dwordx4 v82, s[20:23], s50 offen offset:3072 sc1 lds
  s_waitcnt lgkmcnt(0)
  v_mfma_f32_16x16x32_bf16 v[8:11], a[60:63], v[36:39], v[8:11]
  v_mfma_f32_16x16x32_bf16 v[12:15], a[124:127], v[36:39], v[12:15]
  s_nop 6
  v_cvt_pk_bf16_f32 v44, v8, v9
  v_cvt_pk_bf16_f32 v45, v10, v11
  v_cvt_pk_bf16_f32 v46, v12, v13
  v_cvt_pk_bf16_f32 v47, v14, v15
  ds_write_b128 v89, v[44:47] offset:4096
  s_waitcnt lgkmcnt(0)
  s_barrier
  .if DBGT
  s_memrealtime s[72:73]
  s_lshl_b32 s74, s44, 2
  s_or_b32 s74, s74, 2
  s_waitcnt lgkmcnt(0)
  s_lshl_b32 s72, s72, 10
  s_or_b32 s72, s72, s74
  v_mov_b32 v88, s72
  buffer_store_dword v88, v87, s[20:23], s70 offen
  s_add_u32 s70, s70, 4
  .endif
  s_waitcnt vmcnt(7)
  s_call_b64 s[66:67], .Lqa_fin_%=
  s_branch .Lqa_cont2_%=
.Lqa_end_%=:
  s_waitcnt vmcnt(0) lgkmcnt(0)
  s_endpgm
)ASM"
    : "+{v[0:3]}"(m0), "+{v[4:7]}"(m1), "+{v[78:81]}"(xb), "+{s26}"(o0), "+{s27}"(o1), "+{s28}"(n)
    : "{s[20:23]}"(srd), "{s24}"(w), "{s25}"(h * 256), "{s[4:11]}"(go),
      "{s[72:75]}"(make_srd(p.wq, WBYTES)), "{s[76:77]}"(wrot(p, rr)), "{s[78:79]}"(wrot(p, rr + 1)), "{s[80:81]}"(wrot(p, rr + 2)), "{s84}"(rr), "{s86}"(w ? OOB : OFF_DBG + 896 * KB + (h * 3 + r) * KB),
      "{v8}"((4 * g + w) * 8192 + sh_col(w, c, g)), "{v9}"(sh_rd(w, c, g)), "{s29}"(r), "{s30}"(OFF_H + w * 16 * KB + (w & 1) * 8 * KB), "{s87}"(OFF_H + w * 16 * KB + (fb ^ HX)),
      "{s31}"(OFF_KV + sp((h * 4 + w) * 4 * KB) + (fb ^ KVX)), "{s32}"(OFF_KVM + sp(h * 4 * KB) + (fb ^ 8 * KB)), "{s33}"(OFF_OX + sp((mu * 128 + h * 4 + 2 * eh) * KB) + fb),
      "{s34}"(fkv + z0 + r * 16), "{s98}"(fkv + z1 + r1 * 16), "{s35}"(fb + sp(F_O + (h * 3 + r) * 128 + w * 4)), "{s2}"(fb + sp(F_O + (96 + h * 3 + r1) * 128 + w * 4)), "{s3}"(r1),
      "{s69}"(OFF_DBG + 192 * KB + ((h * 3 + r) * 4 + w) * 64), "{v82}"(lane * 16), "{v83}"(pa), "{v85}"(g * 128 + (c >> 1) * 16), "{v86}"(2 * KB + eh * KB + ((lane * 8) ^ ((g >> 1) * 64))), "{v87}"(lane ? OOB : 0u)
    : ASM_TOP);
  __builtin_amdgcn_endpgm();
}

DEVI void opj_role(const Params& p, const int w, const int L, const int kq, const int nq, const u32x8 go) {
  const u32 fb = 0;
  const int lane = lane_id();
  const int c = lane & 15, g = lane >> 4;
  const u32x4 srd = make_srd(p.ws, WS_USED);
  u32 src = OFF_OX + sp((kq * 16 + w * 4) * KB) + fb;
  u32 dst = OFF_PO + (L ? 0 : LSH) + sp((kq * 2 + (w & 1)) * KB) + (2 * nq + (w >> 1)) * 256 + fb;
  asm volatile(DBGT ".set SLP, " STR(SLP) "\n.set WDLY, " STR(WDLY) "\n.set HSL, " STR(HSL) "\n.set LSH, " STR(LSHM) " << 20\n" R"ASM(
  s_lshl_b32 s38, s24, 12
  s_mov_b32 s30, 0
  s_mov_b32 s31, 0
  s_mov_b32 s32, 0
  s_mov_b32 s34, 0
  .rept 4
  s_sleep WDLY
  .endr
  s_lshl_b32 s36, s24, 15
  s_add_u32 s36, s36, 32768
  s_mov_b32 s72, 0
  s_mov_b32 s73, 0
  s_getpc_b64 s[70:71]
.Lopj_pc_%=:
  s_add_u32 s70, s70, .Lopj_ck_%=-.Lopj_pc_%=
  s_addc_u32 s71, s71, 0
.Lopj_dsub_%=:
  s_cmp_ge_u32 s72, 8
  s_cbranch_scc1 .Lopj_dskip_%=
  s_mov_b32 s74, s72
  s_lshl_b32 s35, s74, 16
  s_add_u32 s35, s35, s28
  s_mov_b32 s74, 7168
  s_add_u32 m0, s36, s73
  s_waitcnt vmcnt(16) lgkmcnt(0)
  s_barrier
  s_cmp_lg_u32 s72, 4
  s_cbranch_scc1 .Lopj_dgo_%=
  s_add_u32 s7, s7, 16
  v_mov_b32 v36, s7
  v_mov_b32 v37, -1
  s_mov_b64 exec, 1
  buffer_store_dwordx2 v[36:37], off, s[20:23], s6 sc1
  s_mov_b64 exec, -1
.Lopj_dgo_%=:
  buffer_load_dwordx4 v10, s[56:59], s35 offen offset:0 )ASM" WPOL R"ASM( lds
  s_add_u32 s35, s35, s74
  buffer_load_dwordx4 v10, s[56:59], s35 offen offset:1024 )ASM" WPOL R"ASM( lds
  s_add_u32 s35, s35, s74
  buffer_load_dwordx4 v10, s[56:59], s35 offen offset:2048 )ASM" WPOL R"ASM( lds
  s_add_u32 s35, s35, s74
  buffer_load_dwordx4 v10, s[56:59], s35 offen offset:3072 )ASM" WPOL R"ASM( lds
  s_add_u32 m0, m0, 4096
  s_add_u32 s35, s35, 4096
  s_add_u32 s35, s35, s74
  buffer_load_dwordx4 v10, s[56:59], s35 offen offset:0 )ASM" WPOL R"ASM( lds
  s_add_u32 s35, s35, s74
  buffer_load_dwordx4 v10, s[56:59], s35 offen offset:1024 )ASM" WPOL R"ASM( lds
  s_add_u32 s35, s35, s74
  buffer_load_dwordx4 v10, s[56:59], s35 offen offset:2048 )ASM" WPOL R"ASM( lds
  s_add_u32 s35, s35, s74
  buffer_load_dwordx4 v10, s[56:59], s35 offen offset:3072 )ASM" WPOL R"ASM( lds
.Lopj_dnext_%=:
  s_add_u32 s73, s73, 8192
  s_and_b32 s73, s73, 24576
  s_add_u32 s72, s72, 1
  s_sub_u32 s74, s72, 4
  s_cbranch_scc1 .Lopj_dsub_%=
  v_add_u32 v12, s73, v11
  s_cmp_ge_u32 s74, 8
  s_cbranch_scc1 .Lopj_dexit_%=
  s_mul_i32 s74, s74, 132
  s_add_u32 s68, s70, s74
  s_addc_u32 s69, s71, 0
  s_setpc_b64 s[68:69]
.Lopj_dskip_%=:
  s_waitcnt vmcnt(0)
  s_barrier
  s_branch .Lopj_dnext_%=
.Lopj_ck_%=:
  ds_read_b64_tr_b16 a[0:1], v12 offset:0
  ds_read_b64_tr_b16 a[64:65], v12 offset:8
  ds_read_b64_tr_b16 a[128:129], v12 offset:16
  ds_read_b64_tr_b16 a[192:193], v12 offset:24
  ds_read_b64_tr_b16 a[2:3], v12 offset:2048
  ds_read_b64_tr_b16 a[66:67], v12 offset:2056
  ds_read_b64_tr_b16 a[130:131], v12 offset:2064
  ds_read_b64_tr_b16 a[194:195], v12 offset:2072
  ds_read_b64_tr_b16 a[4:5], v12 offset:4096
  ds_read_b64_tr_b16 a[68:69], v12 offset:4104
  ds_read_b64_tr_b16 a[132:133], v12 offset:4112
  ds_read_b64_tr_b16 a[196:197], v12 offset:4120
  ds_read_b64_tr_b16 a[6:7], v12 offset:6144
  ds_read_b64_tr_b16 a[70:71], v12 offset:6152
  ds_read_b64_tr_b16 a[134:135], v12 offset:6160
  ds_read_b64_tr_b16 a[198:199], v12 offset:6168
  s_branch .Lopj_dsub_%=
  ds_read_b64_tr_b16 a[8:9], v12 offset:0
  ds_read_b64_tr_b16 a[72:73], v12 offset:8
  ds_read_b64_tr_b16 a[136:137], v12 offset:16
  ds_read_b64_tr_b16 a[200:201], v12 offset:24
  ds_read_b64_tr_b16 a[10:11], v12 offset:2048
  ds_read_b64_tr_b16 a[74:75], v12 offset:2056
  ds_read_b64_tr_b16 a[138:139], v12 offset:2064
  ds_read_b64_tr_b16 a[202:203], v12 offset:2072
  ds_read_b64_tr_b16 a[12:13], v12 offset:4096
  ds_read_b64_tr_b16 a[76:77], v12 offset:4104
  ds_read_b64_tr_b16 a[140:141], v12 offset:4112
  ds_read_b64_tr_b16 a[204:205], v12 offset:4120
  ds_read_b64_tr_b16 a[14:15], v12 offset:6144
  ds_read_b64_tr_b16 a[78:79], v12 offset:6152
  ds_read_b64_tr_b16 a[142:143], v12 offset:6160
  ds_read_b64_tr_b16 a[206:207], v12 offset:6168
  s_branch .Lopj_dsub_%=
  ds_read_b64_tr_b16 a[16:17], v12 offset:0
  ds_read_b64_tr_b16 a[80:81], v12 offset:8
  ds_read_b64_tr_b16 a[144:145], v12 offset:16
  ds_read_b64_tr_b16 a[208:209], v12 offset:24
  ds_read_b64_tr_b16 a[18:19], v12 offset:2048
  ds_read_b64_tr_b16 a[82:83], v12 offset:2056
  ds_read_b64_tr_b16 a[146:147], v12 offset:2064
  ds_read_b64_tr_b16 a[210:211], v12 offset:2072
  ds_read_b64_tr_b16 a[20:21], v12 offset:4096
  ds_read_b64_tr_b16 a[84:85], v12 offset:4104
  ds_read_b64_tr_b16 a[148:149], v12 offset:4112
  ds_read_b64_tr_b16 a[212:213], v12 offset:4120
  ds_read_b64_tr_b16 a[22:23], v12 offset:6144
  ds_read_b64_tr_b16 a[86:87], v12 offset:6152
  ds_read_b64_tr_b16 a[150:151], v12 offset:6160
  ds_read_b64_tr_b16 a[214:215], v12 offset:6168
  s_branch .Lopj_dsub_%=
  ds_read_b64_tr_b16 a[24:25], v12 offset:0
  ds_read_b64_tr_b16 a[88:89], v12 offset:8
  ds_read_b64_tr_b16 a[152:153], v12 offset:16
  ds_read_b64_tr_b16 a[216:217], v12 offset:24
  ds_read_b64_tr_b16 a[26:27], v12 offset:2048
  ds_read_b64_tr_b16 a[90:91], v12 offset:2056
  ds_read_b64_tr_b16 a[154:155], v12 offset:2064
  ds_read_b64_tr_b16 a[218:219], v12 offset:2072
  ds_read_b64_tr_b16 a[28:29], v12 offset:4096
  ds_read_b64_tr_b16 a[92:93], v12 offset:4104
  ds_read_b64_tr_b16 a[156:157], v12 offset:4112
  ds_read_b64_tr_b16 a[220:221], v12 offset:4120
  ds_read_b64_tr_b16 a[30:31], v12 offset:6144
  ds_read_b64_tr_b16 a[94:95], v12 offset:6152
  ds_read_b64_tr_b16 a[158:159], v12 offset:6160
  ds_read_b64_tr_b16 a[222:223], v12 offset:6168
  s_branch .Lopj_dsub_%=
  ds_read_b64_tr_b16 a[32:33], v12 offset:0
  ds_read_b64_tr_b16 a[96:97], v12 offset:8
  ds_read_b64_tr_b16 a[160:161], v12 offset:16
  ds_read_b64_tr_b16 a[224:225], v12 offset:24
  ds_read_b64_tr_b16 a[34:35], v12 offset:2048
  ds_read_b64_tr_b16 a[98:99], v12 offset:2056
  ds_read_b64_tr_b16 a[162:163], v12 offset:2064
  ds_read_b64_tr_b16 a[226:227], v12 offset:2072
  ds_read_b64_tr_b16 a[36:37], v12 offset:4096
  ds_read_b64_tr_b16 a[100:101], v12 offset:4104
  ds_read_b64_tr_b16 a[164:165], v12 offset:4112
  ds_read_b64_tr_b16 a[228:229], v12 offset:4120
  ds_read_b64_tr_b16 a[38:39], v12 offset:6144
  ds_read_b64_tr_b16 a[102:103], v12 offset:6152
  ds_read_b64_tr_b16 a[166:167], v12 offset:6160
  ds_read_b64_tr_b16 a[230:231], v12 offset:6168
  s_branch .Lopj_dsub_%=
  ds_read_b64_tr_b16 a[40:41], v12 offset:0
  ds_read_b64_tr_b16 a[104:105], v12 offset:8
  ds_read_b64_tr_b16 a[168:169], v12 offset:16
  ds_read_b64_tr_b16 a[232:233], v12 offset:24
  ds_read_b64_tr_b16 a[42:43], v12 offset:2048
  ds_read_b64_tr_b16 a[106:107], v12 offset:2056
  ds_read_b64_tr_b16 a[170:171], v12 offset:2064
  ds_read_b64_tr_b16 a[234:235], v12 offset:2072
  ds_read_b64_tr_b16 a[44:45], v12 offset:4096
  ds_read_b64_tr_b16 a[108:109], v12 offset:4104
  ds_read_b64_tr_b16 a[172:173], v12 offset:4112
  ds_read_b64_tr_b16 a[236:237], v12 offset:4120
  ds_read_b64_tr_b16 a[46:47], v12 offset:6144
  ds_read_b64_tr_b16 a[110:111], v12 offset:6152
  ds_read_b64_tr_b16 a[174:175], v12 offset:6160
  ds_read_b64_tr_b16 a[238:239], v12 offset:6168
  s_branch .Lopj_dsub_%=
  ds_read_b64_tr_b16 a[48:49], v12 offset:0
  ds_read_b64_tr_b16 a[112:113], v12 offset:8
  ds_read_b64_tr_b16 a[176:177], v12 offset:16
  ds_read_b64_tr_b16 a[240:241], v12 offset:24
  ds_read_b64_tr_b16 a[50:51], v12 offset:2048
  ds_read_b64_tr_b16 a[114:115], v12 offset:2056
  ds_read_b64_tr_b16 a[178:179], v12 offset:2064
  ds_read_b64_tr_b16 a[242:243], v12 offset:2072
  ds_read_b64_tr_b16 a[52:53], v12 offset:4096
  ds_read_b64_tr_b16 a[116:117], v12 offset:4104
  ds_read_b64_tr_b16 a[180:181], v12 offset:4112
  ds_read_b64_tr_b16 a[244:245], v12 offset:4120
  ds_read_b64_tr_b16 a[54:55], v12 offset:6144
  ds_read_b64_tr_b16 a[118:119], v12 offset:6152
  ds_read_b64_tr_b16 a[182:183], v12 offset:6160
  ds_read_b64_tr_b16 a[246:247], v12 offset:6168
  s_branch .Lopj_dsub_%=
  ds_read_b64_tr_b16 a[56:57], v12 offset:0
  ds_read_b64_tr_b16 a[120:121], v12 offset:8
  ds_read_b64_tr_b16 a[184:185], v12 offset:16
  ds_read_b64_tr_b16 a[248:249], v12 offset:24
  ds_read_b64_tr_b16 a[58:59], v12 offset:2048
  ds_read_b64_tr_b16 a[122:123], v12 offset:2056
  ds_read_b64_tr_b16 a[186:187], v12 offset:2064
  ds_read_b64_tr_b16 a[250:251], v12 offset:2072
  ds_read_b64_tr_b16 a[60:61], v12 offset:4096
  ds_read_b64_tr_b16 a[124:125], v12 offset:4104
  ds_read_b64_tr_b16 a[188:189], v12 offset:4112
  ds_read_b64_tr_b16 a[252:253], v12 offset:4120
  ds_read_b64_tr_b16 a[62:63], v12 offset:6144
  ds_read_b64_tr_b16 a[126:127], v12 offset:6152
  ds_read_b64_tr_b16 a[190:191], v12 offset:6160
  ds_read_b64_tr_b16 a[254:255], v12 offset:6168
  s_branch .Lopj_dsub_%=
.Lopj_dexit_%=:
  buffer_load_dword v36, off, s[20:23], s5 sc1
  s_waitcnt lgkmcnt(0)
  s_branch .Lopj_go1_%=
.Lopj_go0_%=:
  s_and_b32 vcc_lo, s34, 15
  s_cbranch_scc1 .Lopj_go2_%=
  s_add_u32 s7, s7, 16
  v_mov_b32 v36, s7
  v_mov_b32 v37, -1
  s_mov_b64 exec, 1
  buffer_store_dwordx2 v[36:37], off, s[20:23], s6 sc1
  s_mov_b64 exec, -1
.Lopj_go2_%=:
  s_add_u32 s34, s34, 1
  s_cmp_gt_u32 s34, 0x40000
  s_cbranch_scc1 .Lopj_end_%=
  s_sleep 2
  buffer_load_dword v36, off, s[20:23], s5 sc1
.Lopj_go1_%=:
  s_waitcnt vmcnt(0)
  v_readfirstlane_b32 vcc_lo, v36
  s_nop 3
  s_cmp_eq_u32 vcc_lo, s4
  s_cbranch_scc1 .Lopj_go0_%=
  s_mov_b32 s4, vcc_lo
  s_bitcmp0_b32 s4, 2
  s_cbranch_scc1 .Lopj_go3_%=
  s_lshl_b32 s2, s24, 6
  v_mbcnt_lo_u32_b32 v0, -1, s2
  v_mbcnt_hi_u32_b32 v0, -1, v0
  s_mov_b64 s[0:1], s[8:9]
  s_mov_b32 s2, s10
  s_waitcnt vmcnt(0) lgkmcnt(0)
  s_barrier
  s_branch _ZN3rmt11main_kernelENS_6ParamsE
.Lopj_go3_%=:
  s_lshl_b32 s35, s4, 13
  s_and_b32 s35, s35, 0x2000
  s_xor_b32 s25, s25, s35
  s_xor_b32 s26, s26, s35
  s_xor_b32 s27, s27, s35
  v_xor_b32 v1, s35, v1
  v_xor_b32 v14, s35, v14
  v_readlane_b32 s12, v2, 63
  .if DBGT
  s_memrealtime s[56:57]
  s_waitcnt lgkmcnt(0)
  v_mov_b32 v28, s56
  s_mov_b64 exec, 1
  buffer_store_dword v28, off, s[20:23], s29 offset:60
  s_mov_b64 exec, -1
  .endif
  s_branch .Lopj_poll_%=
.Lopj_abort_%=:
  v_mov_b32 v7, 1
  buffer_store_dword v7, v3, s[20:23], 0 offen offset:0 sc1
  s_branch .Lopj_end_%=
.Lopj_late_%=:
  s_add_u32 s96, s96, 1
  s_add_u32 s35, s30, 3
  s_min_u32 s35, s35, s32
  s_and_b32 s36, s31, 3
  s_lshl_b32 s36, s36, 14
  s_add_u32 s36, s36, s38
  s_cmp_lt_u32 s31, s35
  s_cselect_b32 s37, s25, 0xf0000000
  s_cselect_b32 m0, s36, 0x30000
  s_cselect_b32 s33, s31, -1
  s_cselect_b32 s36, 262144, 0
  s_addc_u32 s31, s31, 0
  s_add_u32 s35, s25, s36
  s_xor_b32 s25, s25, s35
  s_lshr_b32 s25, s25, 8
  s_and_b32 s25, s25, 0x2000
  s_xor_b32 s25, s25, s35
  buffer_load_dwordx4 v0, s[20:23], s37 offen offset:0 sc1 lds
  buffer_load_dwordx4 v0, s[20:23], s37 offen offset:1024 sc1 lds
  buffer_load_dwordx4 v0, s[20:23], s37 offen offset:2048 sc1 lds
  buffer_load_dwordx4 v0, s[20:23], s37 offen offset:3072 sc1 lds
  s_waitcnt vmcnt(5)
  s_branch .Lopj_fast_%=
.Lopj_slow_%=:
  s_nop 7
  s_add_u32 s97, s97, 1
  v_cvt_pk_bf16_f32 v32, v16, v20
  v_cvt_pk_bf16_f32 v33, v24, v28
  v_cvt_pk_bf16_f32 v34, v17, v21
  v_cvt_pk_bf16_f32 v35, v25, v29
  buffer_store_dwordx4 v[32:35], v4, s[20:23], s26 offen offset:0 sc1
  v_cvt_pk_bf16_f32 v36, v18, v22
  v_cvt_pk_bf16_f32 v37, v26, v30
  v_cvt_pk_bf16_f32 v38, v19, v23
  v_cvt_pk_bf16_f32 v39, v27, v31
  buffer_store_dwordx4 v[36:39], v5, s[20:23], s26 offen offset:0 sc1
  s_add_u32 s35, s26, 262144
  s_xor_b32 s26, s26, s35
  s_lshr_b32 s26, s26, 8
  s_and_b32 s26, s26, 0x2000
  s_xor_b32 s26, s26, s35
  s_cmp_lt_u32 s31, s32
  s_cbranch_scc1 .Lopj_issue_%=
  buffer_load_dword v8, v1, s[20:23], 0 offen offset:0 sc1
  s_waitcnt vmcnt(1)
  v_lshl_add_u32 v7, s30, 1, 0
  buffer_store_dword v7, v3, s[20:23], s27 offen offset:0 sc1
  s_waitcnt vmcnt(1)
  s_cmp_eq_u32 s30, s12
  s_cbranch_scc0 .Lopj_ev_%=
  s_cmp_eq_u32 s30, 128
  s_cbranch_scc0 .Lopj_end_%=
  s_mov_b32 s12, 129
  v_add_u32 v2, 1, v2
  s_mov_b32 s36, -(LSH + 262144)
  s_add_u32 s35, s26, s36
  s_xor_b32 s26, s26, s35
  s_lshr_b32 s26, s26, 8
  s_and_b32 s26, s26, 0x2000
  s_xor_b32 s26, s26, s35
  s_mov_b32 s36, -262144
  s_add_u32 s35, s25, s36
  s_xor_b32 s25, s25, s35
  s_lshr_b32 s25, s25, 8
  s_and_b32 s25, s25, 0x2000
  s_xor_b32 s25, s25, s35
  v_mov_b32 v1, v14
  .rept HSL
  s_sleep 127
  .endr
.Lopj_poll_%=:
  s_add_u32 s34, s34, 1
  s_cmp_gt_u32 s34, 0x40000
  s_cbranch_scc1 .Lopj_abort_%=
  s_sleep SLP
  buffer_load_dword v8, v1, s[20:23], 0 offen offset:0 sc1
  s_waitcnt vmcnt(0)
.Lopj_ev_%=:
  v_add_u32 v8, v8, v2
  v_cmp_ge_u32 vcc, s31, v8
  s_add_u32 s35, s31, 1
  v_cmp_ge_u32 s[40:41], s35, v8
  s_cmp_eq_u64 vcc, 0
  s_addc_u32 s32, s31, 0
  s_cmp_eq_u64 s[40:41], 0
  s_addc_u32 s32, s32, 0
  s_cmp_lt_u32 s31, s32
  s_cbranch_scc0 .Lopj_poll_%=
.Lopj_issue_%=:
  s_add_u32 s35, s30, 2
  s_min_u32 s35, s35, s32
  s_and_b32 s36, s31, 3
  s_lshl_b32 s36, s36, 14
  s_add_u32 s36, s36, s38
  s_cmp_lt_u32 s31, s35
  s_cselect_b32 s37, s25, 0xf0000000
  s_cselect_b32 m0, s36, 0x30000
  s_cselect_b32 s33, s31, -1
  s_cselect_b32 s36, 262144, 0
  s_addc_u32 s31, s31, 0
  s_add_u32 s35, s25, s36
  s_xor_b32 s25, s25, s35
  s_lshr_b32 s25, s25, 8
  s_and_b32 s25, s25, 0x2000
  s_xor_b32 s25, s25, s35
  buffer_load_dwordx4 v0, s[20:23], s37 offen offset:0 sc1 lds
  buffer_load_dwordx4 v0, s[20:23], s37 offen offset:1024 sc1 lds
  buffer_load_dwordx4 v0, s[20:23], s37 offen offset:2048 sc1 lds
  buffer_load_dwordx4 v0, s[20:23], s37 offen offset:3072 sc1 lds
  s_add_u32 s35, s30, 2
  s_min_u32 s35, s35, s32
  s_and_b32 s36, s31, 3
  s_lshl_b32 s36, s36, 14
  s_add_u32 s36, s36, s38
  s_cmp_lt_u32 s31, s35
  s_cselect_b32 s37, s25, 0xf0000000
  s_cselect_b32 m0, s36, 0x30000
  s_cselect_b32 s33, s31, -1
  s_cselect_b32 s36, 262144, 0
  s_addc_u32 s31, s31, 0
  s_add_u32 s35, s25, s36
  s_xor_b32 s25, s25, s35
  s_lshr_b32 s25, s25, 8
  s_and_b32 s25, s25, 0x2000
  s_xor_b32 s25, s25, s35
  buffer_load_dwordx4 v0, s[20:23], s37 offen offset:0 sc1 lds
  buffer_load_dwordx4 v0, s[20:23], s37 offen offset:1024 sc1 lds
  buffer_load_dwordx4 v0, s[20:23], s37 offen offset:2048 sc1 lds
  buffer_load_dwordx4 v0, s[20:23], s37 offen offset:3072 sc1 lds
  s_and_b32 s35, s30, 3
  s_lshl_b32 s35, s35, 14
  v_add_u32 v6, s35, v0
  s_waitcnt vmcnt(8)
  v_lshl_add_u32 v7, s30, 1, 0
  buffer_store_dword v7, v3, s[20:23], s27 offen offset:0 sc1
  s_waitcnt vmcnt(5)
  s_barrier
  ds_read_b128 v[40:43], v6 offset:0
  ds_read_b128 v[44:47], v6 offset:1024
  ds_read_b128 v[48:51], v6 offset:2048
.Lopj_body_%=:
  s_waitcnt lgkmcnt(2)
  v_mfma_f32_16x16x32_bf16 v[16:19], v[40:43], a[0:3], 0
  ds_read_b128 v[52:55], v6 offset:3072
  s_add_u32 s35, s30, 4
  v_mfma_f32_16x16x32_bf16 v[20:23], v[40:43], a[64:67], 0
  s_min_u32 s35, s35, s32
  s_and_b32 s36, s31, 3
  v_mfma_f32_16x16x32_bf16 v[24:27], v[40:43], a[128:131], 0
  s_lshl_b32 s36, s36, 14
  s_add_u32 s36, s36, s38
  v_mfma_f32_16x16x32_bf16 v[28:31], v[40:43], a[192:195], 0
  s_cmp_lt_u32 s31, s35
  s_cselect_b32 s37, s25, 0xf0000000
  s_waitcnt lgkmcnt(2)
  v_mfma_f32_16x16x32_bf16 v[16:19], v[44:47], a[4:7], v[16:19]
  ds_read_b128 v[40:43], v6 offset:4096
  s_cselect_b32 m0, s36, 0x30000
  v_mfma_f32_16x16x32_bf16 v[20:23], v[44:47], a[68:71], v[20:23]
  s_cselect_b32 s33, s31, -1
  s_cselect_b32 s36, 262144, 0
  v_mfma_f32_16x16x32_bf16 v[24:27], v[44:47], a[132:135], v[24:27]
  s_addc_u32 s31, s31, 0
  s_add_u32 s35, s25, s36
  v_mfma_f32_16x16x32_bf16 v[28:31], v[44:47], a[196:199], v[28:31]
  s_xor_b32 s25, s25, s35
  s_lshr_b32 s25, s25, 8
  s_waitcnt lgkmcnt(2)
  v_mfma_f32_16x16x32_bf16 v[16:19], v[48:51], a[8:11], v[16:19]
  ds_read_b128 v[44:47], v6 offset:5120
  s_and_b32 s25, s25, 0x2000
  v_mfma_f32_16x16x32_bf16 v[20:23], v[48:51], a[72:75], v[20:23]
  s_xor_b32 s25, s25, s35
  buffer_load_dword v8, v1, s[20:23], 0 offen offset:0 sc1
  v_mfma_f32_16x16x32_bf16 v[24:27], v[48:51], a[136:139], v[24:27]
  v_mfma_f32_16x16x32_bf16 v[28:31], v[48:51], a[200:203], v[28:31]
  s_waitcnt lgkmcnt(2)
  v_mfma_f32_16x16x32_bf16 v[16:19], v[52:55], a[12:15], v[16:19]
  ds_read_b128 v[48:51], v6 offset:6144
  v_mfma_f32_16x16x32_bf16 v[20:23], v[52:55], a[76:79], v[20:23]
  buffer_load_dwordx4 v0, s[20:23], s37 offen offset:0 sc1 lds
  v_mfma_f32_16x16x32_bf16 v[24:27], v[52:55], a[140:143], v[24:27]
  v_mfma_f32_16x16x32_bf16 v[28:31], v[52:55], a[204:207], v[28:31]
  s_waitcnt lgkmcnt(2)
  v_mfma_f32_16x16x32_bf16 v[16:19], v[40:43], a[16:19], v[16:19]
  ds_read_b128 v[52:55], v6 offset:7168
  v_mfma_f32_16x16x32_bf16 v[20:23], v[40:43], a[80:83], v[20:23]
  buffer_load_dwordx4 v0, s[20:23], s37 offen offset:1024 sc1 lds
  v_mfma_f32_16x16x32_bf16 v[24:27], v[40:43], a[144:147], v[24:27]
  v_mfma_f32_16x16x32_bf16 v[28:31], v[40:43], a[208:211], v[28:31]
  s_waitcnt lgkmcnt(2)
  v_mfma_f32_16x16x32_bf16 v[16:19], v[44:47], a[20:23], v[16:19]
  ds_read_b128 v[40:43], v6 offset:8192
  v_mfma_f32_16x16x32_bf16 v[20:23], v[44:47], a[84:87], v[20:23]
  buffer_load_dwordx4 v0, s[20:23], s37 offen offset:2048 sc1 lds
  v_mfma_f32_16x16x32_bf16 v[24:27], v[44:47], a[148:151], v[24:27]
  v_mfma_f32_16x16x32_bf16 v[28:31], v[44:47], a[212:215], v[28:31]
  s_waitcnt lgkmcnt(2)
  v_mfma_f32_16x16x32_bf16 v[16:19], v[48:51], a[24:27], v[16:19]
  ds_read_b128 v[44:47], v6 offset:9216
  v_mfma_f32_16x16x32_bf16 v[20:23], v[48:51], a[88:91], v[20:23]
  buffer_load_dwordx4 v0, s[20:23], s37 offen offset:3072 sc1 lds
  v_mfma_f32_16x16x32_bf16 v[24:27], v[48:51], a[152:155], v[24:27]
  v_mfma_f32_16x16x32_bf16 v[28:31], v[48:51], a[216:219], v[28:31]
  s_waitcnt lgkmcnt(2)
  v_mfma_f32_16x16x32_bf16 v[16:19], v[52:55], a[28:31], v[16:19]
  ds_read_b128 v[48:51], v6 offset:10240
  v_mfma_f32_16x16x32_bf16 v[20:23], v[52:55], a[92:95], v[20:23]
  v_mfma_f32_16x16x32_bf16 v[24:27], v[52:55], a[156:159], v[24:27]
  v_mfma_f32_16x16x32_bf16 v[28:31], v[52:55], a[220:223], v[28:31]
  s_waitcnt lgkmcnt(2)
  v_mfma_f32_16x16x32_bf16 v[16:19], v[40:43], a[32:35], v[16:19]
  ds_read_b128 v[52:55], v6 offset:11264
  v_mfma_f32_16x16x32_bf16 v[20:23], v[40:43], a[96:99], v[20:23]
  v_mfma_f32_16x16x32_bf16 v[24:27], v[40:43], a[160:163], v[24:27]
  v_mfma_f32_16x16x32_bf16 v[28:31], v[40:43], a[224:227], v[28:31]
  s_waitcnt lgkmcnt(2)
  v_mfma_f32_16x16x32_bf16 v[16:19], v[44:47], a[36:39], v[16:19]
  ds_read_b128 v[40:43], v6 offset:12288
  v_mfma_f32_16x16x32_bf16 v[20:23], v[44:47], a[100:103], v[20:23]
  v_mfma_f32_16x16x32_bf16 v[24:27], v[44:47], a[164:167], v[24:27]
  v_mfma_f32_16x16x32_bf16 v[28:31], v[44:47], a[228:231], v[28:31]
  s_waitcnt lgkmcnt(2)
  v_mfma_f32_16x16x32_bf16 v[16:19], v[48:51], a[40:43], v[16:19]
  ds_read_b128 v[44:47], v6 offset:13312
  v_mfma_f32_16x16x32_bf16 v[20:23], v[48:51], a[104:107], v[20:23]
  v_mfma_f32_16x16x32_bf16 v[24:27], v[48:51], a[168:171], v[24:27]
  v_mfma_f32_16x16x32_bf16 v[28:31], v[48:51], a[232:235], v[28:31]
  s_waitcnt lgkmcnt(2)
  v_mfma_f32_16x16x32_bf16 v[16:19], v[52:55], a[44:47], v[16:19]
  ds_read_b128 v[48:51], v6 offset:14336
  v_mfma_f32_16x16x32_bf16 v[20:23], v[52:55], a[108:111], v[20:23]
  v_mfma_f32_16x16x32_bf16 v[24:27], v[52:55], a[172:175], v[24:27]
  s_waitcnt vmcnt(4)
  v_lshl_add_u32 v7, s30, 1, 1
  v_mfma_f32_16x16x32_bf16 v[28:31], v[52:55], a[236:239], v[28:31]
  buffer_store_dword v7, v3, s[20:23], s27 offen offset:0 sc1
  v_add_u32 v8, v8, v2
  s_waitcnt lgkmcnt(2)
  v_mfma_f32_16x16x32_bf16 v[16:19], v[40:43], a[48:51], v[16:19]
  ds_read_b128 v[52:55], v6 offset:15360
  v_cmp_ge_u32 vcc, s31, v8
  v_mfma_f32_16x16x32_bf16 v[20:23], v[40:43], a[112:115], v[20:23]
  s_add_u32 s35, s31, 1
  v_cmp_ge_u32 s[40:41], s35, v8
  v_mfma_f32_16x16x32_bf16 v[24:27], v[40:43], a[176:179], v[24:27]
  s_cmp_eq_u64 vcc, 0
  s_addc_u32 s32, s31, 0
  v_mfma_f32_16x16x32_bf16 v[28:31], v[40:43], a[240:243], v[28:31]
  s_cmp_eq_u64 s[40:41], 0
  s_addc_u32 s32, s32, 0
  s_waitcnt lgkmcnt(2)
  v_mfma_f32_16x16x32_bf16 v[16:19], v[44:47], a[52:55], v[16:19]
  v_mfma_f32_16x16x32_bf16 v[20:23], v[44:47], a[116:119], v[20:23]
  v_mfma_f32_16x16x32_bf16 v[24:27], v[44:47], a[180:183], v[24:27]
  v_mfma_f32_16x16x32_bf16 v[28:31], v[44:47], a[244:247], v[28:31]
  s_waitcnt lgkmcnt(1)
  v_mfma_f32_16x16x32_bf16 v[16:19], v[48:51], a[56:59], v[16:19]
  v_mfma_f32_16x16x32_bf16 v[20:23], v[48:51], a[120:123], v[20:23]
  v_mfma_f32_16x16x32_bf16 v[24:27], v[48:51], a[184:187], v[24:27]
  s_add_u32 s30, s30, 1
  s_and_b32 s35, s30, 3
  v_mfma_f32_16x16x32_bf16 v[28:31], v[48:51], a[248:251], v[28:31]
  s_lshl_b32 s35, s35, 14
  v_add_u32 v6, s35, v0
  s_waitcnt lgkmcnt(0)
  v_mfma_f32_16x16x32_bf16 v[16:19], v[52:55], a[60:63], v[16:19]
  v_mfma_f32_16x16x32_bf16 v[20:23], v[52:55], a[124:127], v[20:23]
  v_mfma_f32_16x16x32_bf16 v[24:27], v[52:55], a[188:191], v[24:27]
  v_mfma_f32_16x16x32_bf16 v[28:31], v[52:55], a[252:255], v[28:31]
  s_cmp_eq_u32 s31, s30
  s_cbranch_scc1 .Lopj_slow_%=
  s_cmp_eq_u32 s33, s30
  s_cbranch_scc1 .Lopj_late_%=
.Lopj_fast_%=:
  s_barrier
  ds_read_b128 v[40:43], v6 offset:0
  ds_read_b128 v[44:47], v6 offset:1024
  ds_read_b128 v[48:51], v6 offset:2048
  v_cvt_pk_bf16_f32 v32, v16, v20
  v_cvt_pk_bf16_f32 v33, v24, v28
  v_cvt_pk_bf16_f32 v34, v17, v21
  v_cvt_pk_bf16_f32 v35, v25, v29
  buffer_store_dwordx4 v[32:35], v4, s[20:23], s26 offen offset:0 sc1
  v_cvt_pk_bf16_f32 v36, v18, v22
  v_cvt_pk_bf16_f32 v37, v26, v30
  v_cvt_pk_bf16_f32 v38, v19, v23
  v_cvt_pk_bf16_f32 v39, v27, v31
  buffer_store_dwordx4 v[36:39], v5, s[20:23], s26 offen offset:0 sc1
  s_add_u32 s35, s26, 262144
  s_xor_b32 s26, s26, s35
  s_lshr_b32 s26, s26, 8
  s_and_b32 s26, s26, 0x2000
  s_xor_b32 s26, s26, s35
  s_branch .Lopj_body_%=
.Lopj_end_%=:
  s_waitcnt vmcnt(0) lgkmcnt(0)
  s_endpgm
)ASM"
    : "+{s25}"(src), "+{s26}"(dst)
    : "{s[20:23]}"(srd), "{s24}"(w), "{s[4:11]}"(go), "{s27}"(fb + sp(F_PO) + (L * 16 + kq * 2 + nq) * 256 + w * 4),
      "{s28}"(kq * 512 * KB + nq * 512 + w * 128), "{s[56:59]}"(make_srd(p.wo, WBYTES)), "{v10}"((lane >> 3) * KB + (lane & 7) * 16), "{v11}"((w + 1) * 32 * KB + (lane >> 2) * 128 + (c & 3) * 32),
      "{s29}"(OFF_DBG + 64 * KB + ((L * 16 + kq * 2 + nq) * 4 + w) * 64), "{s86}"(w | kq | nq ? OOB : OFF_DBG + (860 + L * 4) * KB),
      "{v0}"(lane * 16), "{v1}"(g == 3 ? OOB : fb + sp(F_O + ((L * 32 + kq * 4 + (c >> 2)) * 3 + g) * 128 + (c & 3) * 4)), "{v14}"(g == 3 ? OOB : fb + sp(F_O + ((32 + kq * 4 + (c >> 2)) * 3 + g) * 128 + (c & 3) * 4)), "{v2}"(g == 3 ? 128 - L : g * 2 + ((c >> 1) & 1)), "{v3}"(lane ? OOB : 0u), "{v4}"(g * 64 * KB + c * 16), "{v5}"(g * 64 * KB + 32 * KB + c * 16)
    : ASM_TOP);
  __builtin_amdgcn_endpgm();
}

DEVI void ffn_role(const Params& p, const int w, const int L, const int j, const u32x8 go) {
  const u32 fb = 0;
  const int lane = lane_id();
  const int c = lane & 15, g = lane >> 4;
  const u32x4 srd = make_srd(p.ws, WS_USED);
  const int hb = 4 * j + w;
  const bool hv = hb < NFB;
  u32 src = OFF_H1 + sp(((u32)L * SEQ + w * 4) * KB) + fb;
  u32 dst = OFF_PF + (L ? 0 : LSH) + sp(j * 2 * KB) + w * 256 + fb;
  u32x4 ro, w1o, w2o; f32x4 b0, b1;
  const u32x4 wl = {(u32)(4 * g + w) * (FF * 2) + sh_col(w, c, g), sh_rd(w, c, g), g * KB + c * 16, (w + 1) * 32 * KB + (lane >> 2) * 256 + (c & 3) * 32};
#pragma unroll
  for (int i = 0; i < 4; ++i) {
    ro[i] = c * KB + ((4 * i + g) ^ c) * 16;
    w1o[i] = RF(j * 256 + i * 128 * FF * 2);
    w2o[i] = RF(4 * j + (w ^ i) < NFB ? (4 * j + (w ^ i)) * 32 * KB + w * 256 : OOB);
    b0[i] = hv ? (float)p.b1[sigma(hb, g, i)] : 0.f; b1[i] = hv ? (float)p.b1[sigma(hb, g, 4 + i)] : 0.f;
  }
  asm volatile(DBGT ".set SLP, " STR(SLP) "\n.set WDLY, " STR(WDLY) "\n.set HSL, " STR(HSL) "\n.set LSH, " STR(LSHM) " << 20\n" R"ASM(
  s_lshl_b32 s38, s24, 12
  s_mov_b32 s39, 0xf0000000
  s_mov_b32 s30, 0
  s_mov_b32 s31, 0
  s_mov_b32 s32, 0
  s_mov_b32 s34, 0
  v_xor_b32 v27, 32, v21
  .rept 4
  s_sleep WDLY
  .endr
  s_lshl_b32 s36, s24, 15
  s_add_u32 s36, s36, 32768
  s_mov_b32 s72, 0
  s_mov_b32 s73, 0
  s_getpc_b64 s[70:71]
.Lffn_pc_%=:
  s_add_u32 s70, s70, .Lffn_ck_%=-.Lffn_pc_%=
  s_addc_u32 s71, s71, 0
.Lffn_dsub_%=:
  s_cmp_ge_u32 s72, 8
  s_cbranch_scc1 .Lffn_dskip_%=
  s_mov_b32 s74, s72
  s_mov_b32 m0, s74
  s_cmp_ge_u32 s74, 4
  s_cselect_b64 s[56:57], s[62:63], s[60:61]
  s_cselect_b64 vcc, -1, 0
  s_movrels_b32 s35, s44
  s_cselect_b32 s74, 3, 42
  v_cndmask_b32 v24, v20, v22, vcc
  s_lshl_b32 s74, s74, 10
  s_add_u32 m0, s36, s73
  s_waitcnt vmcnt(16) lgkmcnt(0)
  s_barrier
  s_cmp_lg_u32 s72, 4
  s_cbranch_scc1 .Lffn_dgo_%=
  s_add_u32 s7, s7, 16
  v_mov_b32 v36, s7
  v_mov_b32 v37, -1
  s_mov_b64 exec, 1
  buffer_store_dwordx2 v[36:37], off, s[20:23], s6 sc1
  s_mov_b64 exec, -1
.Lffn_dgo_%=:
  buffer_load_dwordx4 v24, s[56:59], s35 offen offset:0 )ASM" WPOL R"ASM( lds
  s_add_u32 s35, s35, s74
  buffer_load_dwordx4 v24, s[56:59], s35 offen offset:1024 )ASM" WPOL R"ASM( lds
  s_add_u32 s35, s35, s74
  buffer_load_dwordx4 v24, s[56:59], s35 offen offset:2048 )ASM" WPOL R"ASM( lds
  s_add_u32 s35, s35, s74
  buffer_load_dwordx4 v24, s[56:59], s35 offen offset:3072 )ASM" WPOL R"ASM( lds
  s_add_u32 m0, m0, 4096
  s_add_u32 s35, s35, 4096
  s_add_u32 s35, s35, s74
  buffer_load_dwordx4 v24, s[56:59], s35 offen offset:0 )ASM" WPOL R"ASM( lds
  s_add_u32 s35, s35, s74
  buffer_load_dwordx4 v24, s[56:59], s35 offen offset:1024 )ASM" WPOL R"ASM( lds
  s_add_u32 s35, s35, s74
  buffer_load_dwordx4 v24, s[56:59], s35 offen offset:2048 )ASM" WPOL R"ASM( lds
  s_add_u32 s35, s35, s74
  buffer_load_dwordx4 v24, s[56:59], s35 offen offset:3072 )ASM" WPOL R"ASM( lds
.Lffn_dnext_%=:
  s_add_u32 s73, s73, 8192
  s_and_b32 s73, s73, 24576
  s_add_u32 s72, s72, 1
  s_sub_u32 s74, s72, 4
  s_cbranch_scc1 .Lffn_dsub_%=
  v_add_u32 v25, s73, v21
  v_add_u32 v26, s73, v23
  v_add_u32 v28, s73, v27
  s_cmp_ge_u32 s74, 8
  s_cbranch_scc1 .Lffn_dexit_%=
  s_mul_i32 s74, s74, 132
  s_add_u32 s68, s70, s74
  s_addc_u32 s69, s71, 0
  s_setpc_b64 s[68:69]
.Lffn_dskip_%=:
  s_waitcnt vmcnt(0)
  s_barrier
  s_branch .Lffn_dnext_%=
.Lffn_ck_%=:
  ds_read_b64_tr_b16 a[0:1], v25 offset:0
  ds_read_b64_tr_b16 a[64:65], v28 offset:0
  ds_read_b64_tr_b16 a[4:5], v25 offset:1024
  ds_read_b64_tr_b16 a[68:69], v28 offset:1024
  ds_read_b64_tr_b16 a[8:9], v25 offset:2048
  ds_read_b64_tr_b16 a[72:73], v28 offset:2048
  ds_read_b64_tr_b16 a[12:13], v25 offset:3072
  ds_read_b64_tr_b16 a[76:77], v28 offset:3072
  ds_read_b64_tr_b16 a[2:3], v25 offset:4096
  ds_read_b64_tr_b16 a[66:67], v28 offset:4096
  ds_read_b64_tr_b16 a[6:7], v25 offset:5120
  ds_read_b64_tr_b16 a[70:71], v28 offset:5120
  ds_read_b64_tr_b16 a[10:11], v25 offset:6144
  ds_read_b64_tr_b16 a[74:75], v28 offset:6144
  ds_read_b64_tr_b16 a[14:15], v25 offset:7168
  ds_read_b64_tr_b16 a[78:79], v28 offset:7168
  s_branch .Lffn_dsub_%=
  ds_read_b64_tr_b16 a[16:17], v25 offset:0
  ds_read_b64_tr_b16 a[80:81], v28 offset:0
  ds_read_b64_tr_b16 a[20:21], v25 offset:1024
  ds_read_b64_tr_b16 a[84:85], v28 offset:1024
  ds_read_b64_tr_b16 a[24:25], v25 offset:2048
  ds_read_b64_tr_b16 a[88:89], v28 offset:2048
  ds_read_b64_tr_b16 a[28:29], v25 offset:3072
  ds_read_b64_tr_b16 a[92:93], v28 offset:3072
  ds_read_b64_tr_b16 a[18:19], v25 offset:4096
  ds_read_b64_tr_b16 a[82:83], v28 offset:4096
  ds_read_b64_tr_b16 a[22:23], v25 offset:5120
  ds_read_b64_tr_b16 a[86:87], v28 offset:5120
  ds_read_b64_tr_b16 a[26:27], v25 offset:6144
  ds_read_b64_tr_b16 a[90:91], v28 offset:6144
  ds_read_b64_tr_b16 a[30:31], v25 offset:7168
  ds_read_b64_tr_b16 a[94:95], v28 offset:7168
  s_branch .Lffn_dsub_%=
  ds_read_b64_tr_b16 a[32:33], v25 offset:0
  ds_read_b64_tr_b16 a[96:97], v28 offset:0
  ds_read_b64_tr_b16 a[36:37], v25 offset:1024
  ds_read_b64_tr_b16 a[100:101], v28 offset:1024
  ds_read_b64_tr_b16 a[40:41], v25 offset:2048
  ds_read_b64_tr_b16 a[104:105], v28 offset:2048
  ds_read_b64_tr_b16 a[44:45], v25 offset:3072
  ds_read_b64_tr_b16 a[108:109], v28 offset:3072
  ds_read_b64_tr_b16 a[34:35], v25 offset:4096
  ds_read_b64_tr_b16 a[98:99], v28 offset:4096
  ds_read_b64_tr_b16 a[38:39], v25 offset:5120
  ds_read_b64_tr_b16 a[102:103], v28 offset:5120
  ds_read_b64_tr_b16 a[42:43], v25 offset:6144
  ds_read_b64_tr_b16 a[106:107], v28 offset:6144
  ds_read_b64_tr_b16 a[46:47], v25 offset:7168
  ds_read_b64_tr_b16 a[110:111], v28 offset:7168
  s_branch .Lffn_dsub_%=
  ds_read_b64_tr_b16 a[48:49], v25 offset:0
  ds_read_b64_tr_b16 a[112:113], v28 offset:0
  ds_read_b64_tr_b16 a[52:53], v25 offset:1024
  ds_read_b64_tr_b16 a[116:117], v28 offset:1024
  ds_read_b64_tr_b16 a[56:57], v25 offset:2048
  ds_read_b64_tr_b16 a[120:121], v28 offset:2048
  ds_read_b64_tr_b16 a[60:61], v25 offset:3072
  ds_read_b64_tr_b16 a[124:125], v28 offset:3072
  ds_read_b64_tr_b16 a[50:51], v25 offset:4096
  ds_read_b64_tr_b16 a[114:115], v28 offset:4096
  ds_read_b64_tr_b16 a[54:55], v25 offset:5120
  ds_read_b64_tr_b16 a[118:119], v28 offset:5120
  ds_read_b64_tr_b16 a[58:59], v25 offset:6144
  ds_read_b64_tr_b16 a[122:123], v28 offset:6144
  ds_read_b64_tr_b16 a[62:63], v25 offset:7168
  ds_read_b64_tr_b16 a[126:127], v28 offset:7168
  s_branch .Lffn_dsub_%=
  ds_read_b64_tr_b16 a[128:129], v26 offset:0
  ds_read_b64_tr_b16 a[144:145], v26 offset:8
  ds_read_b64_tr_b16 a[160:161], v26 offset:16
  ds_read_b64_tr_b16 a[176:177], v26 offset:24
  ds_read_b64_tr_b16 a[192:193], v26 offset:128
  ds_read_b64_tr_b16 a[208:209], v26 offset:136
  ds_read_b64_tr_b16 a[224:225], v26 offset:144
  ds_read_b64_tr_b16 a[240:241], v26 offset:152
  ds_read_b64_tr_b16 a[130:131], v26 offset:4096
  ds_read_b64_tr_b16 a[146:147], v26 offset:4104
  ds_read_b64_tr_b16 a[162:163], v26 offset:4112
  ds_read_b64_tr_b16 a[178:179], v26 offset:4120
  ds_read_b64_tr_b16 a[194:195], v26 offset:4224
  ds_read_b64_tr_b16 a[210:211], v26 offset:4232
  ds_read_b64_tr_b16 a[226:227], v26 offset:4240
  ds_read_b64_tr_b16 a[242:243], v26 offset:4248
  s_branch .Lffn_dsub_%=
  ds_read_b64_tr_b16 a[132:133], v26 offset:0
  ds_read_b64_tr_b16 a[148:149], v26 offset:8
  ds_read_b64_tr_b16 a[164:165], v26 offset:16
  ds_read_b64_tr_b16 a[180:181], v26 offset:24
  ds_read_b64_tr_b16 a[196:197], v26 offset:128
  ds_read_b64_tr_b16 a[212:213], v26 offset:136
  ds_read_b64_tr_b16 a[228:229], v26 offset:144
  ds_read_b64_tr_b16 a[244:245], v26 offset:152
  ds_read_b64_tr_b16 a[134:135], v26 offset:4096
  ds_read_b64_tr_b16 a[150:151], v26 offset:4104
  ds_read_b64_tr_b16 a[166:167], v26 offset:4112
  ds_read_b64_tr_b16 a[182:183], v26 offset:4120
  ds_read_b64_tr_b16 a[198:199], v26 offset:4224
  ds_read_b64_tr_b16 a[214:215], v26 offset:4232
  ds_read_b64_tr_b16 a[230:231], v26 offset:4240
  ds_read_b64_tr_b16 a[246:247], v26 offset:4248
  s_branch .Lffn_dsub_%=
  ds_read_b64_tr_b16 a[136:137], v26 offset:0
  ds_read_b64_tr_b16 a[152:153], v26 offset:8
  ds_read_b64_tr_b16 a[168:169], v26 offset:16
  ds_read_b64_tr_b16 a[184:185], v26 offset:24
  ds_read_b64_tr_b16 a[200:201], v26 offset:128
  ds_read_b64_tr_b16 a[216:217], v26 offset:136
  ds_read_b64_tr_b16 a[232:233], v26 offset:144
  ds_read_b64_tr_b16 a[248:249], v26 offset:152
  ds_read_b64_tr_b16 a[138:139], v26 offset:4096
  ds_read_b64_tr_b16 a[154:155], v26 offset:4104
  ds_read_b64_tr_b16 a[170:171], v26 offset:4112
  ds_read_b64_tr_b16 a[186:187], v26 offset:4120
  ds_read_b64_tr_b16 a[202:203], v26 offset:4224
  ds_read_b64_tr_b16 a[218:219], v26 offset:4232
  ds_read_b64_tr_b16 a[234:235], v26 offset:4240
  ds_read_b64_tr_b16 a[250:251], v26 offset:4248
  s_branch .Lffn_dsub_%=
  ds_read_b64_tr_b16 a[140:141], v26 offset:0
  ds_read_b64_tr_b16 a[156:157], v26 offset:8
  ds_read_b64_tr_b16 a[172:173], v26 offset:16
  ds_read_b64_tr_b16 a[188:189], v26 offset:24
  ds_read_b64_tr_b16 a[204:205], v26 offset:128
  ds_read_b64_tr_b16 a[220:221], v26 offset:136
  ds_read_b64_tr_b16 a[236:237], v26 offset:144
  ds_read_b64_tr_b16 a[252:253], v26 offset:152
  ds_read_b64_tr_b16 a[142:143], v26 offset:4096
  ds_read_b64_tr_b16 a[158:159], v26 offset:4104
  ds_read_b64_tr_b16 a[174:175], v26 offset:4112
  ds_read_b64_tr_b16 a[190:191], v26 offset:4120
  ds_read_b64_tr_b16 a[206:207], v26 offset:4224
  ds_read_b64_tr_b16 a[222:223], v26 offset:4232
  ds_read_b64_tr_b16 a[238:239], v26 offset:4240
  ds_read_b64_tr_b16 a[254:255], v26 offset:4248
  s_branch .Lffn_dsub_%=
.Lffn_dexit_%=:
  buffer_load_dword v36, off, s[20:23], s5 sc1
  s_waitcnt lgkmcnt(0)
  s_branch .Lffn_go1_%=
.Lffn_go0_%=:
  s_and_b32 vcc_lo, s34, 15
  s_cbranch_scc1 .Lffn_go2_%=
  s_add_u32 s7, s7, 16
  v_mov_b32 v36, s7
  v_mov_b32 v37, -1
  s_mov_b64 exec, 1
  buffer_store_dwordx2 v[36:37], off, s[20:23], s6 sc1
  s_mov_b64 exec, -1
.Lffn_go2_%=:
  s_add_u32 s34, s34, 1
  s_cmp_gt_u32 s34, 0x40000
  s_cbranch_scc1 .Lffn_end_%=
  s_sleep 2
  buffer_load_dword v36, off, s[20:23], s5 sc1
.Lffn_go1_%=:
  s_waitcnt vmcnt(0)
  v_readfirstlane_b32 vcc_lo, v36
  s_nop 3
  s_cmp_eq_u32 vcc_lo, s4
  s_cbranch_scc1 .Lffn_go0_%=
  s_mov_b32 s4, vcc_lo
  s_bitcmp0_b32 s4, 2
  s_cbranch_scc1 .Lffn_go3_%=
  s_lshl_b32 s2, s24, 6
  v_mbcnt_lo_u32_b32 v0, -1, s2
  v_mbcnt_hi_u32_b32 v0, -1, v0
  s_mov_b64 s[0:1], s[8:9]
  s_mov_b32 s2, s10
  s_waitcnt vmcnt(0) lgkmcnt(0)
  s_barrier
  s_branch _ZN3rmt11main_kernelENS_6ParamsE
.Lffn_go3_%=:
  s_lshl_b32 s35, s4, 13
  s_and_b32 s35, s35, 0x2000
  s_xor_b32 s25, s25, s35
  s_xor_b32 s26, s26, s35
  s_xor_b32 s27, s27, s35
  v_xor_b32 v1, s35, v1
  v_xor_b32 v167, s35, v167
  v_readlane_b32 s12, v2, 63
  s_lshl_b32 s35, s24, 10
  s_add_u32 s35, s35, 65536
  v_or_b32 v24, s35, v0
  v_xor_b32 v25, 1024, v24
  v_xor_b32 v26, 2048, v24
  v_xor_b32 v27, 3072, v24
  v_mov_b32 v7, 0xc0135761
  s_mov_b32 s43, 0xbdd2d3e8
  .if DBGT
  s_memrealtime s[56:57]
  s_waitcnt lgkmcnt(0)
  v_mov_b32 v28, s56
  s_mov_b64 exec, 1
  buffer_store_dword v28, off, s[20:23], s29 offset:60
  s_mov_b64 exec, -1
  .endif
  s_branch .Lffn_poll_%=
.Lffn_abort_%=:
  v_mov_b32 v164, 1
  buffer_store_dword v164, v3, s[20:23], 0 offen offset:0 sc1
  s_branch .Lffn_end_%=
.Lffn_latea_%=:
  s_add_u32 s96, s96, 1
  s_waitcnt vmcnt(6)
  s_branch .Lffn_fast_%=
.Lffn_lateb_%=:
  s_add_u32 s96, s96, 1
  s_waitcnt vmcnt(2)
  s_branch .Lffn_fast_%=
.Lffn_slow_%=:
  s_nop 7
  s_add_u32 s97, s97, 1
  v_cvt_pk_bf16_f32 v92, v76, v80
  v_cvt_pk_bf16_f32 v93, v84, v88
  v_cvt_pk_bf16_f32 v94, v77, v81
  v_cvt_pk_bf16_f32 v95, v85, v89
  buffer_store_dwordx4 v[92:95], v4, s[20:23], s39 offen offset:1024 sc1
  v_cvt_pk_bf16_f32 v96, v78, v82
  v_cvt_pk_bf16_f32 v97, v86, v90
  v_cvt_pk_bf16_f32 v98, v79, v83
  v_cvt_pk_bf16_f32 v99, v87, v91
  buffer_store_dwordx4 v[96:99], v5, s[20:23], s39 offen offset:1024 sc1
  s_mov_b32 s39, 0xf0000000
  buffer_load_dword v6, v1, s[20:23], 0 offen offset:0 sc1
  s_waitcnt vmcnt(1)
  v_lshl_add_u32 v164, s30, 1, 0
  buffer_store_dword v164, v3, s[20:23], s27 offen offset:0 sc1
  s_waitcnt vmcnt(1)
  s_cmp_eq_u32 s30, s12
  s_cbranch_scc0 .Lffn_ev_%=
  s_cmp_eq_u32 s30, 128
  s_cbranch_scc0 .Lffn_end_%=
  s_mov_b32 s12, 129
  v_add_u32 v2, 1, v2
  s_mov_b32 s36, -(LSH + 524288)
  s_add_u32 s35, s26, s36
  s_xor_b32 s26, s26, s35
  s_lshr_b32 s26, s26, 8
  s_and_b32 s26, s26, 0x2000
  s_xor_b32 s26, s26, s35
  s_add_u32 s25, s25, 0x400000
  s_xor_b32 s25, s25, 0x7fa000
  v_add_u32 v1, 2048, v1
  .rept HSL
  s_sleep 127
  .endr
.Lffn_poll_%=:
  s_add_u32 s34, s34, 1
  s_cmp_gt_u32 s34, 0x40000
  s_cbranch_scc1 .Lffn_abort_%=
  s_sleep SLP
  buffer_load_dword v6, v1, s[20:23], 0 offen offset:0 sc1
  s_waitcnt vmcnt(0)
.Lffn_ev_%=:
  v_add_u32 v6, v6, v2
  v_cmp_ge_u32 vcc, s31, v6
  s_add_u32 s35, s31, 1
  v_cmp_ge_u32 s[40:41], s35, v6
  s_cmp_eq_u64 vcc, 0
  s_addc_u32 s32, s31, 0
  s_cmp_eq_u64 s[40:41], 0
  s_addc_u32 s32, s32, 0
  s_cmp_lt_u32 s31, s32
  s_cbranch_scc0 .Lffn_poll_%=
  s_add_u32 s42, s30, 2
  s_mul_i32 s53, s31, 0x8080
  s_and_b32 s53, s53, 0xffffa000
  s_xor_b32 s53, s53, s25
  s_and_b32 s54, s31, 3
  s_lshl_b32 s54, s54, 14
  s_add_u32 s54, s54, s38
  s_min_u32 s35, s42, s32
  s_cmp_lt_u32 s31, s35
  s_cselect_b32 s37, s53, 0xf0000000
  s_cselect_b32 m0, s54, 0x30000
  s_cselect_b32 s33, s31, -1
  s_addc_u32 s31, s31, 0
  buffer_load_dwordx4 v0, s[20:23], s37 offen offset:0 sc1 lds
  buffer_load_dwordx4 v0, s[20:23], s37 offen offset:1024 sc1 lds
  buffer_load_dwordx4 v0, s[20:23], s37 offen offset:2048 sc1 lds
  buffer_load_dwordx4 v0, s[20:23], s37 offen offset:3072 sc1 lds
  s_mul_i32 s53, s31, 0x8080
  s_and_b32 s53, s53, 0xffffa000
  s_xor_b32 s53, s53, s25
  s_and_b32 s54, s31, 3
  s_lshl_b32 s54, s54, 14
  s_add_u32 s54, s54, s38
  s_min_u32 s35, s42, s32
  s_cmp_lt_u32 s31, s35
  s_cselect_b32 s37, s53, 0xf0000000
  s_cselect_b32 m0, s54, 0x30000
  s_cselect_b32 s33, s31, -1
  s_addc_u32 s31, s31, 0
  buffer_load_dwordx4 v0, s[20:23], s37 offen offset:0 sc1 lds
  buffer_load_dwordx4 v0, s[20:23], s37 offen offset:1024 sc1 lds
  buffer_load_dwordx4 v0, s[20:23], s37 offen offset:2048 sc1 lds
  buffer_load_dwordx4 v0, s[20:23], s37 offen offset:3072 sc1 lds
  s_and_b32 s35, s30, 3
  s_lshl_b32 s35, s35, 14
  v_add_u32 v20, s35, v8
  v_add_u32 v21, s35, v9
  v_add_u32 v22, s35, v10
  v_add_u32 v23, s35, v11
  s_waitcnt vmcnt(4)
.Lffn_fast_%=:
  s_barrier
  ds_read_b128 v[100:103], v20 offset:0
  ds_read_b128 v[104:107], v21 offset:0
  ds_read_b128 v[108:111], v22 offset:0
  ds_read_b128 v[112:115], v23 offset:0
  ds_read_b128 v[116:119], v20 offset:256
  ds_read_b128 v[120:123], v21 offset:256
  v_cvt_pk_bf16_f32 v92, v76, v80
  v_cvt_pk_bf16_f32 v93, v84, v88
  v_cvt_pk_bf16_f32 v94, v77, v81
  v_cvt_pk_bf16_f32 v95, v85, v89
  v_cvt_pk_bf16_f32 v96, v78, v82
  v_cvt_pk_bf16_f32 v97, v86, v90
  v_cvt_pk_bf16_f32 v98, v79, v83
  v_cvt_pk_bf16_f32 v99, v87, v91
  s_waitcnt lgkmcnt(5)
  v_mfma_f32_16x16x32_bf16 v[28:31], a[0:3], v[100:103], v[12:15]
  buffer_store_dwordx4 v[92:95], v4, s[20:23], s39 offen offset:1024 sc1
  s_waitcnt lgkmcnt(4)
  v_mfma_f32_16x16x32_bf16 v[28:31], a[4:7], v[104:107], v[28:31]
  ds_read_b128 v[124:127], v22 offset:256
  s_waitcnt lgkmcnt(4)
  v_mfma_f32_16x16x32_bf16 v[28:31], a[8:11], v[108:111], v[28:31]
  buffer_store_dwordx4 v[96:99], v5, s[20:23], s39 offen offset:1024 sc1
  s_waitcnt lgkmcnt(3)
  v_mfma_f32_16x16x32_bf16 v[28:31], a[12:15], v[112:115], v[28:31]
  ds_read_b128 v[128:131], v23 offset:256
  s_waitcnt lgkmcnt(3)
  v_mfma_f32_16x16x32_bf16 v[28:31], a[16:19], v[116:119], v[28:31]
  ds_read_b128 v[132:135], v20 offset:512
  s_waitcnt lgkmcnt(3)
  v_mfma_f32_16x16x32_bf16 v[28:31], a[20:23], v[120:123], v[28:31]
  ds_read_b128 v[136:139], v21 offset:512
  s_waitcnt lgkmcnt(3)
  v_mfma_f32_16x16x32_bf16 v[28:31], a[24:27], v[124:127], v[28:31]
  ds_read_b128 v[140:143], v22 offset:512
  s_waitcnt lgkmcnt(3)
  v_mfma_f32_16x16x32_bf16 v[28:31], a[28:31], v[128:131], v[28:31]
  ds_read_b128 v[144:147], v23 offset:512
  s_waitcnt lgkmcnt(3)
  v_mfma_f32_16x16x32_bf16 v[28:31], a[32:35], v[132:135], v[28:31]
  ds_read_b128 v[148:151], v20 offset:768
  s_waitcnt lgkmcnt(3)
  v_mfma_f32_16x16x32_bf16 v[28:31], a[36:39], v[136:139], v[28:31]
  ds_read_b128 v[152:155], v21 offset:768
  s_waitcnt lgkmcnt(3)
  v_mfma_f32_16x16x32_bf16 v[28:31], a[40:43], v[140:143], v[28:31]
  ds_read_b128 v[156:159], v22 offset:768
  s_waitcnt lgkmcnt(3)
  v_mfma_f32_16x16x32_bf16 v[28:31], a[44:47], v[144:147], v[28:31]
  ds_read_b128 v[160:163], v23 offset:768
  s_waitcnt lgkmcnt(3)
  v_mfma_f32_16x16x32_bf16 v[28:31], a[48:51], v[148:151], v[28:31]
  s_waitcnt lgkmcnt(2)
  v_mfma_f32_16x16x32_bf16 v[28:31], a[52:55], v[152:155], v[28:31]
  buffer_load_dword v6, v1, s[20:23], 0 offen offset:0 sc1
  s_waitcnt lgkmcnt(1)
  v_mfma_f32_16x16x32_bf16 v[28:31], a[56:59], v[156:159], v[28:31]
  v_lshl_add_u32 v164, s30, 1, 1
  s_waitcnt lgkmcnt(0)
  v_mfma_f32_16x16x32_bf16 v[28:31], a[60:63], v[160:163], v[28:31]
  s_mul_i32 s53, s31, 0x8080
  s_and_b32 s53, s53, 0xffffa000
  v_mfma_f32_16x16x32_bf16 v[32:35], a[64:67], v[100:103], v[16:19]
  s_xor_b32 s53, s53, s25
  s_and_b32 s54, s31, 3
  v_mfma_f32_16x16x32_bf16 v[32:35], a[68:71], v[104:107], v[32:35]
  s_lshl_b32 s54, s54, 14
  s_add_u32 s54, s54, s38
  v_mfma_f32_16x16x32_bf16 v[32:35], a[72:75], v[108:111], v[32:35]
  s_add_u32 s42, s30, 4
  v_mul_f32 v36, v28, v28
  v_mul_f32 v37, v29, v29
  v_mfma_f32_16x16x32_bf16 v[32:35], a[76:79], v[112:115], v[32:35]
  s_mov_b32 s39, s26
  v_mul_f32 v38, v30, v30
  v_mul_f32 v39, v31, v31
  v_mfma_f32_16x16x32_bf16 v[32:35], a[80:83], v[116:119], v[32:35]
  s_add_u32 s35, s26, 524288
  v_fma_f32 v36, v36, s43, v7
  v_fma_f32 v37, v37, s43, v7
  v_mfma_f32_16x16x32_bf16 v[32:35], a[84:87], v[120:123], v[32:35]
  s_xor_b32 s26, s26, s35
  v_fma_f32 v38, v38, s43, v7
  v_fma_f32 v39, v39, s43, v7
  v_mfma_f32_16x16x32_bf16 v[32:35], a[88:91], v[124:127], v[32:35]
  s_lshr_b32 s26, s26, 8
  v_mul_f32 v36, v36, v28
  v_mul_f32 v37, v37, v29
  v_mfma_f32_16x16x32_bf16 v[32:35], a[92:95], v[128:131], v[32:35]
  s_and_b32 s26, s26, 0x2000
  v_mul_f32 v38, v38, v30
  v_mul_f32 v39, v39, v31
  v_mfma_f32_16x16x32_bf16 v[32:35], a[96:99], v[132:135], v[32:35]
  s_xor_b32 s26, s26, s35
  v_exp_f32 v36, v36
  v_mfma_f32_16x16x32_bf16 v[32:35], a[100:103], v[136:139], v[32:35]
  v_exp_f32 v37, v37
  v_mfma_f32_16x16x32_bf16 v[32:35], a[104:107], v[140:143], v[32:35]
  v_exp_f32 v38, v38
  v_mfma_f32_16x16x32_bf16 v[32:35], a[108:111], v[144:147], v[32:35]
  v_exp_f32 v39, v39
  v_mfma_f32_16x16x32_bf16 v[32:35], a[112:115], v[148:151], v[32:35]
  v_add_f32 v36, 1.0, v36
  v_add_f32 v37, 1.0, v37
  v_mfma_f32_16x16x32_bf16 v[32:35], a[116:119], v[152:155], v[32:35]
  v_add_f32 v38, 1.0, v38
  v_add_f32 v39, 1.0, v39
  v_mfma_f32_16x16x32_bf16 v[32:35], a[120:123], v[156:159], v[32:35]
  buffer_load_dword v166, v1, s[20:23], 0 offen offset:0 sc1
  v_rcp_f32 v36, v36
  v_mfma_f32_16x16x32_bf16 v[32:35], a[124:127], v[160:163], v[32:35]
  v_rcp_f32 v37, v37
  v_rcp_f32 v38, v38
  v_rcp_f32 v39, v39
  v_mul_f32 v36, v36, v28
  v_mul_f32 v37, v37, v29
  v_mul_f32 v38, v38, v30
  v_mul_f32 v39, v39, v31
  v_cvt_pk_bf16_f32 v44, v36, v37
  v_cvt_pk_bf16_f32 v45, v38, v39
  v_mul_f32 v40, v32, v32
  v_mul_f32 v41, v33, v33
  v_mul_f32 v42, v34, v34
  v_mul_f32 v43, v35, v35
  v_fma_f32 v40, v40, s43, v7
  v_fma_f32 v41, v41, s43, v7
  v_fma_f32 v42, v42, s43, v7
  v_fma_f32 v43, v43, s43, v7
  v_mul_f32 v40, v40, v32
  v_mul_f32 v41, v41, v33
  v_mul_f32 v42, v42, v34
  v_mul_f32 v43, v43, v35
  v_exp_f32 v40, v40
  v_exp_f32 v41, v41
  v_exp_f32 v42, v42
  v_exp_f32 v43, v43
  v_add_f32 v40, 1.0, v40
  v_add_f32 v41, 1.0, v41
  v_add_f32 v42, 1.0, v42
  v_add_f32 v43, 1.0, v43
  v_rcp_f32 v40, v40
  v_rcp_f32 v41, v41
  v_rcp_f32 v42, v42
  v_rcp_f32 v43, v43
  v_mul_f32 v40, v40, v32
  v_mul_f32 v41, v41, v33
  v_mul_f32 v42, v42, v34
  v_mul_f32 v43, v43, v35
  v_cvt_pk_bf16_f32 v46, v40, v41
  v_cvt_pk_bf16_f32 v47, v42, v43
  ds_write_b128 v24, v[44:47] offset:0
  s_nop 0
  v_mfma_f32_16x16x32_bf16 v[60:63], v[44:47], a[128:131], 0
  s_waitcnt vmcnt(1)
  buffer_store_dword v164, v3, s[20:23], s27 offen offset:0 sc1
  v_mfma_f32_16x16x32_bf16 v[64:67], v[44:47], a[144:147], 0
  s_add_u32 s35, s31, 1
  v_add_u32 v6, v6, v2
  v_cmp_ge_u32 vcc, s31, v6
  v_mfma_f32_16x16x32_bf16 v[68:71], v[44:47], a[160:163], 0
  v_cmp_ge_u32 s[40:41], s35, v6
  v_mfma_f32_16x16x32_bf16 v[72:75], v[44:47], a[176:179], 0
  s_waitcnt lgkmcnt(0)
  s_barrier
  ds_read_b128 v[48:51], v25 offset:0
  ds_read_b128 v[52:55], v26 offset:0
  ds_read_b128 v[56:59], v27 offset:0
  v_mfma_f32_16x16x32_bf16 v[76:79], v[44:47], a[192:195], 0
  s_cmp_eq_u64 vcc, 0
  s_addc_u32 s32, s31, 0
  s_cmp_eq_u64 s[40:41], 0
  v_mfma_f32_16x16x32_bf16 v[80:83], v[44:47], a[208:211], 0
  s_addc_u32 s32, s32, 0
  s_min_u32 s35, s42, s32
  s_cmp_lt_u32 s31, s35
  v_mfma_f32_16x16x32_bf16 v[84:87], v[44:47], a[224:227], 0
  s_cselect_b32 s37, s53, 0xf0000000
  s_cselect_b32 m0, s54, 0x30000
  s_cselect_b32 s33, s31, -1
  v_mfma_f32_16x16x32_bf16 v[88:91], v[44:47], a[240:243], 0
  s_addc_u32 s31, s31, 0
  buffer_load_dwordx4 v0, s[20:23], s37 offen offset:0 sc1 lds
  s_waitcnt lgkmcnt(2)
  v_mfma_f32_16x16x32_bf16 v[60:63], v[48:51], a[132:135], v[60:63]
  buffer_load_dwordx4 v0, s[20:23], s37 offen offset:1024 sc1 lds
  s_mul_i32 s53, s31, 0x8080
  v_mfma_f32_16x16x32_bf16 v[64:67], v[48:51], a[148:151], v[64:67]
  buffer_load_dwordx4 v0, s[20:23], s37 offen offset:2048 sc1 lds
  s_and_b32 s53, s53, 0xffffa000
  v_mfma_f32_16x16x32_bf16 v[68:71], v[48:51], a[164:167], v[68:71]
  buffer_load_dwordx4 v0, s[20:23], s37 offen offset:3072 sc1 lds
  s_xor_b32 s53, s53, s25
  v_mfma_f32_16x16x32_bf16 v[72:75], v[48:51], a[180:183], v[72:75]
  s_and_b32 s54, s31, 3
  s_lshl_b32 s54, s54, 14
  s_waitcnt lgkmcnt(1)
  v_mfma_f32_16x16x32_bf16 v[60:63], v[52:55], a[136:139], v[60:63]
  s_add_u32 s54, s54, s38
  s_add_u32 s30, s30, 1
  s_and_b32 s35, s30, 3
  v_mfma_f32_16x16x32_bf16 v[64:67], v[52:55], a[152:155], v[64:67]
  s_lshl_b32 s35, s35, 14
  v_add_u32 v20, s35, v8
  v_add_u32 v21, s35, v9
  v_mfma_f32_16x16x32_bf16 v[68:71], v[52:55], a[168:171], v[68:71]
  v_add_u32 v22, s35, v10
  v_add_u32 v23, s35, v11
  v_mfma_f32_16x16x32_bf16 v[72:75], v[52:55], a[184:187], v[72:75]
  s_add_u32 s35, s31, 1
  s_waitcnt lgkmcnt(0)
  v_mfma_f32_16x16x32_bf16 v[60:63], v[56:59], a[140:143], v[60:63]
  s_waitcnt vmcnt(5)
  v_add_u32 v166, v166, v2
  v_cmp_ge_u32 vcc, s31, v166
  v_mfma_f32_16x16x32_bf16 v[64:67], v[56:59], a[156:159], v[64:67]
  v_cmp_ge_u32 s[40:41], s35, v166
  v_mfma_f32_16x16x32_bf16 v[68:71], v[56:59], a[172:175], v[68:71]
  s_cmp_eq_u64 vcc, 0
  s_addc_u32 s32, s31, 0
  s_cmp_eq_u64 s[40:41], 0
  v_mfma_f32_16x16x32_bf16 v[72:75], v[56:59], a[188:191], v[72:75]
  s_addc_u32 s32, s32, 0
  s_min_u32 s35, s42, s32
  s_cmp_lt_u32 s31, s35
  v_mfma_f32_16x16x32_bf16 v[76:79], v[48:51], a[196:199], v[76:79]
  s_cselect_b32 s37, s53, 0xf0000000
  s_cselect_b32 m0, s54, 0x30000
  s_cselect_b32 s52, s31, -1
  v_mfma_f32_16x16x32_bf16 v[80:83], v[48:51], a[212:215], v[80:83]
  s_addc_u32 s31, s31, 0
  buffer_load_dwordx4 v0, s[20:23], s37 offen offset:0 sc1 lds
  v_mfma_f32_16x16x32_bf16 v[84:87], v[48:51], a[228:231], v[84:87]
  buffer_load_dwordx4 v0, s[20:23], s37 offen offset:1024 sc1 lds
  v_mfma_f32_16x16x32_bf16 v[88:91], v[48:51], a[244:247], v[88:91]
  buffer_load_dwordx4 v0, s[20:23], s37 offen offset:2048 sc1 lds
  v_mfma_f32_16x16x32_bf16 v[76:79], v[52:55], a[200:203], v[76:79]
  buffer_load_dwordx4 v0, s[20:23], s37 offen offset:3072 sc1 lds
  v_mfma_f32_16x16x32_bf16 v[80:83], v[52:55], a[216:219], v[80:83]
  v_cvt_pk_bf16_f32 v92, v60, v64
  v_cvt_pk_bf16_f32 v93, v68, v72
  v_mfma_f32_16x16x32_bf16 v[84:87], v[52:55], a[232:235], v[84:87]
  v_cvt_pk_bf16_f32 v94, v61, v65
  v_cvt_pk_bf16_f32 v95, v69, v73
  v_mfma_f32_16x16x32_bf16 v[88:91], v[52:55], a[248:251], v[88:91]
  v_cvt_pk_bf16_f32 v96, v62, v66
  v_cvt_pk_bf16_f32 v97, v70, v74
  v_mfma_f32_16x16x32_bf16 v[76:79], v[56:59], a[204:207], v[76:79]
  v_cvt_pk_bf16_f32 v98, v63, v67
  v_cvt_pk_bf16_f32 v99, v71, v75
  v_mfma_f32_16x16x32_bf16 v[80:83], v[56:59], a[220:223], v[80:83]
  buffer_store_dwordx4 v[92:95], v4, s[20:23], s39 offen offset:0 sc1
  v_mfma_f32_16x16x32_bf16 v[84:87], v[56:59], a[236:239], v[84:87]
  buffer_store_dwordx4 v[96:99], v5, s[20:23], s39 offen offset:0 sc1
  v_mfma_f32_16x16x32_bf16 v[88:91], v[56:59], a[252:255], v[88:91]
  s_cmp_eq_u32 s31, s30
  s_cbranch_scc1 .Lffn_slow_%=
  s_cmp_eq_u32 s33, s30
  s_cbranch_scc1 .Lffn_latea_%=
  s_cmp_eq_u32 s52, s30
  s_cbranch_scc0 .Lffn_fast_%=
  s_branch .Lffn_lateb_%=
.Lffn_end_%=:
  s_waitcnt vmcnt(0) lgkmcnt(0)
  s_endpgm
)ASM"
    : "+{s25}"(src), "+{s26}"(dst)
    : "{s[20:23]}"(srd), "{s24}"(w), "{s[4:11]}"(go), "{s27}"(fb + sp(F_PF) + (L * 16 + j) * 256 + w * 4),
      "{s[56:59]}"(make_srd(p.w1, FBYTES)), "{s[60:61]}"(p.w1), "{s[62:63]}"(p.w2), "{v[20:23]}"(wl),
      "{s29}"(OFF_DBG + 128 * KB + ((L * 16 + j) * 4 + w) * 64), "{s86}"(w | j ? OOB : OFF_DBG + (868 + L * 4) * KB), "{s[44:47]}"(w1o), "{s[48:51]}"(w2o), "{v0}"(lane * 16), "{v1}"(g > 1 ? OOB : fb + sp(F_X1) + (L * 8 + (lane >> 2)) * 256 + (lane & 3) * 4), "{v2}"(g > 1 ? 128 - L : lane & 3), "{v3}"(lane ? OOB : 0u),
      "{v4}"(g * 96 * KB + c * 16), "{v5}"(g * 96 * KB + 48 * KB + c * 16), "{v[8:11]}"(ro), "{v[12:15]}"(b0), "{v[16:19]}"(b1)
    : ASM_TOP);
  __builtin_amdgcn_endpgm();
}

DEVI float wave_sum(float x) {
  x += dpp_mov<0xB1>(x); x += dpp_mov<0x4E>(x); x += dpp_mov<0x124>(x); x += dpp_mov<0x128>(x);
  return xor32_add(xor16_add(x));
}
#define OAUX 18
#define LAUX 16
#define SAUX 16
#define TAILS 62
#define RSLP SLP
DEVI void mark(const rsrc_t rs, const u32 o, const int n) {
  for (int k = 0; k < 2 * n; ++k) st16c(rs, o + sp(k * KB) + lane_id() * 16, (u32x4){~0u, ~0u, ~0u, ~0u});
}
template <int STAGE>
DEVI void red_role(const Params& p, const int w, const int L, const int id, const u32 fb, const int sb, const int se) {
  constexpr int NSRC = STAGE == 1 ? 8 : NFC;
  const int lane = lane_id();
  const int mu = w & 1, par = w >> 1, tk = mu * 16 + id * 2;
  const bool fin = STAGE == 2 && L == 1, first = STAGE == 1 && L == 0;
  const rsrc_t rs = make_rsrc(p.ws, WS_USED);
  const u32x4 srd = make_srd(p.ws, WS_USED);
  const bool fv = STAGE == 1 || lane < 4 * NFC;
  u32 fdep = fv ? fb + sp(STAGE == 1 ? F_PO : F_PF) + (L * 16 + (lane >> 2)) * 256 + (lane & 3) * 4 : OOB;
  const u32 fout = fb + (STAGE == 1 ? sp(F_X1) + L * 8 * 256 : sp(F_H)) + id * 256 + w * 4;
  if (STAGE == 2 && !fin) st4c(rs, lane < 3 && (lane & 1) != par ? fout + lane * 8 * 256 : OOB, lane + 1);
  const int c = lane & 15, gq = lane >> 4, q = fin ? (c & 3) * 4 + (c >> 2) : c;
  const u32 one = (q >> 2) == gq ? ((q & 1) ? 0x3f800000u : 0x00003f80u) : 0u;
  const u32x4 sl = {(q & 2) ? 0u : one, (q & 2) ? one : 0u, 0u, 0u}, sh = {0u, 0u, sl.x, sl.y};
  f32x4 bl = {0.f, 0.f, 0.f, 0.f}, bh = bl;
  if (STAGE == 2) {
#pragma unroll
    for (int i = 0; i < 4; ++i) { const int col = fin ? pi(c, i, gq) : pi(lane >> 2, lane & 3, i); bl[i] = (float)p.b2[col]; bh[i] = (float)p.b2[col + 64]; }
  }
  const u32 l16 = fin ? c * 64 + gq * 16 : lane * 16;
  u32 spins = 0, rp = par;
  const u32 lsh = (L ? 0 : LSH) + fb;
  const int s0 = L ? TAILS : NSEG;
  auto mark_next = [&](const int s) {
    if (STAGE == 1 || s < TAILS || s >= NSEG) return;
    mark(rs, flip(OFF_PO + ((2 * s + mu) * 8 + id) * 32 * KB + fb), 8);
    mark(rs, flip(OFF_PF + (2 * s + mu) * 512 * KB + id * 48 * KB + fb), NFC);
  };
  for (int s = sb + par; s < se; s += 2) {
    const u32 tok = (u32)L * SEQ + s * 32 + tk, last = L && 2 * s + mu == 2 * NSEG - 1, need = 4 * s + 2 * mu + 1 + 2 * last;
    if (last) fdep -= 16 * 256;
    f32x4 a0[2][2] = {{bl, bl}, {bh, bh}}, a[2][2];
    if (first) {
#pragma unroll
      for (int i = 0; i < 16; ++i) a0[i >> 3][(i >> 2) & 1][i & 3] = (float)p.x[(tok + ((i >> 2) & 1)) * DM + pi(lane >> 2, lane & 3, (i >> 3) * 4 + (i & 3))];
    }
    const u32 po = flip((STAGE == 1 ? OFF_PO + ((2 * s + mu) * 8 + id) * 32 * KB : OFF_PF + (2 * s + mu) * 512 * KB + id * 48 * KB) + lsh);
    const u32 xo = first ? OOB : flip(sp(tok * KB) + fb);
    for (u32 th = need + (s < s0);; th = need + 1) {
      u32 v;
      for (;;) {
        asm volatile("buffer_load_dword %0, %1, %2, 0 offen sc1\n\ts_waitcnt vmcnt(0)" : "=v"(v) : "v"(fdep), "s"(srd) : "memory");
        if (__ballot(fv && v < th) == 0ull) break;
        if (++spins > SPIN_CAP) { st4c(rs, OFF_FLAGS + F_ABORT, 1u); return; }
        __builtin_amdgcn_s_sleep(RSLP);
      }
      u32x4 pv[NSRC + 1][2];
#pragma unroll
      for (int k = 0; k <= NSRC; ++k)
#pragma unroll
        for (int hf = 0; hf < 2; ++hf)
          pv[k][hf] = __builtin_amdgcn_raw_buffer_load_b128(rs, (k ? po + sp((2 * k - 2) * KB) : (STAGE == 1 ? OFF_X0 : OFF_X1) + xo) + hf * KB + l16, 0, LAUX);
      __builtin_amdgcn_sched_barrier(0);
#pragma unroll
      for (int k = 0; k <= NSRC; ++k)
#pragma unroll
        for (int hf = 0; hf < 2; ++hf) { a[hf][0] = mfma32(as_bf8(sl), as_bf8(pv[k][hf]), k ? a[hf][0] : a0[hf][0]); a[hf][1] = mfma32(as_bf8(sh), as_bf8(pv[k][hf]), k ? a[hf][1] : a0[hf][1]); }
      if (th > need || __ballot(__builtin_isunordered(a[0][0][0], a[1][0][0])) == 0ull) break;
    }
    u32x4 xr[2];
#pragma unroll
    for (int hf = 0; hf < 2; ++hf) xr[hf] = (u32x4){pack2(a[hf][0][0], a[hf][0][1]), pack2(a[hf][0][2], a[hf][0][3]), pack2(a[hf][1][0], a[hf][1][1]), pack2(a[hf][1][2], a[hf][1][3])};
    if (fin) {
      const rsrc_t ro = make_rsrc(p.out, SEQ * DM * 2);
      const u32 o = ((s * 32 + tk) * DM + pi(c, 0, gq)) * 2;
#pragma unroll
      for (int hf = 0; hf < 2; ++hf) {
        __builtin_amdgcn_raw_buffer_store_b64((u32x2){xr[hf].x, xr[hf].y}, ro, o + 128 * hf, 0, OAUX);
        __builtin_amdgcn_raw_buffer_store_b64((u32x2){xr[hf].z, xr[hf].w}, ro, o + DM * 2 + 128 * hf, 0, OAUX);
      }
      continue;
    }
    const u32 to = flip(sp((STAGE == 1 ? tok : tok + SEQ) * KB) + fb);
    float x[2][8], mean[2], rstd[2];
#pragma unroll
    for (int t = 0; t < 2; ++t) {
#pragma unroll
      for (int i = 0; i < 4; ++i) { x[t][2 * i] = bf_lo(xr[i >> 1][2 * t + (i & 1)]); x[t][2 * i + 1] = bf_hi(xr[i >> 1][2 * t + (i & 1)]); }
      mean[t] = wave_sum(((x[t][0] + x[t][1]) + (x[t][2] + x[t][3])) + ((x[t][4] + x[t][5]) + (x[t][6] + x[t][7]))) * (1.f / DM);
    }
#pragma unroll
    for (int t = 0; t < 2; ++t) {
      float q2 = 0.f;
#pragma unroll
      for (int i = 0; i < 8; ++i) { x[t][i] -= mean[t]; q2 = fmaf(x[t][i], x[t][i], q2); }
      rstd[t] = __builtin_amdgcn_rsqf(wave_sum(q2) * (1.f / DM) + 1e-5f);
    }
#pragma unroll
    for (int t = 0; t < 2; ++t) {
      const float r = rstd[t];
      const u32x4 hv = {pack2(x[t][0] * r, x[t][1] * r), pack2(x[t][2] * r, x[t][3] * r), pack2(x[t][4] * r, x[t][5] * r), pack2(x[t][6] * r, x[t][7] * r)};
      __builtin_amdgcn_raw_buffer_store_b128(hv, rs, (STAGE == 1 ? OFF_H1 + to : OFF_H + (to ^ HX)) + t * KB + (lane ^ (id * 2 + t)) * 16, 0, SAUX);
    }
#pragma unroll
    for (int hf = 0; hf < 2; ++hf) __builtin_amdgcn_raw_buffer_store_b128(xr[hf], rs, (STAGE == 1 ? OFF_X1 : OFF_X0) + to + hf * KB + l16, 0, SAUX);
    vm_wait0();
    st4c(rs, lane ? OOB : (STAGE == 1 ? fout : fout + rp * 8 * 256), STAGE == 1 ? 2 * (s - par) + 4 : s + 4);
    mark_next(s + 2);
    rp = rp ? rp - 1 : 2;
  }
}

DEVI int chain_id(int b) { return (b >> 7) * 32 + ((b >> 2) & 30) + (b & 1); }
constexpr u32 NHEADS = 0x0255aa00, FHEAD = (0 << 0) | (10 << 5) | (20 << 10) | (25 << 15) | (30 << 20) | (0 << 25);
DEVI int qkva_id(int b) { const int x = b & 7, k = b >> 3; return k < 3 * (int)((NHEADS >> (4 * x)) & 15) ? 3 * (int)((FHEAD >> ((5 * x - 10) & 31)) & 31) + k : -1; }
#define PFI 0xf8
#define LBIT 4
#define XCC_MASK 7
DEVI void early_loads(const Params& p, int b, int w) {
  const bool ch = (b & 6) == 0;
  const int lane = lane_id(), c = lane & 15, g = lane >> 4;
  const int q = qkva_id(b), hq = q * 171 >> 9;
  const u32x4 srd = make_srd(ch ? p.wq : wrot(p, qrot(q - 3 * hq)), WBYTES);
  const u32 vo = (ch ? 8 * (w >> 1) + 4 * (g & 1) + (g & 2) + (w & 1) : 4 * g + w) * 8192 + sh_col(w, c, g);
  u32 so = ((ch ? chain_id(b) : hq) & 31) * 256 + (ch && b >= 128 ? CHX * MB : 0u), n = 24;
  asm volatile("s_mov_b32 m0, %3\n\ts_nop 0\n"
               "1:\n\tbuffer_load_dwordx4 %2, %4, %0 offen " WPOL " lds\n\ts_add_u32 m0, m0, 1024\n\ts_add_u32 %0, %0, 0x20000\n\ts_and_b32 %0, %0, 0x3fffff\n\ts_sub_u32 %1, %1, 1\n\ts_cmp_lg_u32 %1, 0\n\ts_cbranch_scc1 1b\n\t"
               "s_waitcnt vmcnt(24)\n\ts_barrier"
               : "+s"(so), "+s"(n) : "v"(vo), "s"((w + 1) * 32 * KB), "s"(srd) : "memory", "scc");
}
DEVI void leader(const Params& p, u32x4 d, const u32 v, const u32 rnd, const bool lazy) {
  typedef unsigned short u16x2 __attribute__((ext_vector_type(2)));
  const int lane = lane_id();
  const rsrc_t rs = make_rsrc(p.ws, WS_USED);
  const u32 ag = lane < 8 ? OFF_GO + lane * 128 : OOB;
  u32 m = 0, rec = 0;
  for (u32 spins = 0;; ++spins) {
    u32x2 e[4];
#pragma unroll
    for (int i = 0; i < 4; ++i) e[i] = ld8c(rs, (m >> (8 * i)) & 255 ? OOB : OFF_DONE + (i * 64 + lane) * 128);
    if (__ballot(ld4c(rs, ag) != v) != 0ull || spins > SPIN_CAP) return;
    if (!(m & 255)) rec = e[0].y;
    bool all = true;
#pragma unroll
    for (int i = 0; i < 4; ++i) {
      if (!((m >> (8 * i)) & 255) && e[i].x != d[i] && ((e[i].x >> 3) & 1) == rnd) m |= 1u << (8 * i + (e[i].x & 7));
      all &= ((m >> (8 * i)) & 255) != 0;
    }
    if (__ballot(!all) == 0ull) break;
    if (lazy) asm volatile("s_sleep 127" ::: "memory"); else asm volatile("s_sleep 1" ::: "memory");
  }
  u16x2 t = __builtin_bit_cast(u16x2, rec);
#define STEP8(CTRL) t = __builtin_elementwise_min(t, __builtin_bit_cast(u16x2, __builtin_amdgcn_update_dpp(0u, __builtin_bit_cast(unsigned, t), CTRL, 0xf, 0xf, true))); \
  m |= __builtin_amdgcn_update_dpp(0u, m, CTRL, 0xf, 0xf, true)
  STEP8(0xB1); STEP8(0x4E); STEP8(0x141);
  const u32 t8 = __builtin_bit_cast(u32, t);
  auto r = __builtin_amdgcn_permlane16_swap(t8, t8, false, false);
  const u32 t16 = __builtin_bit_cast(u32, __builtin_elementwise_min(__builtin_bit_cast(u16x2, (u32)r[0]), __builtin_bit_cast(u16x2, (u32)r[1])));
  r = __builtin_amdgcn_permlane32_swap(t16, t16, false, false);
  t = __builtin_elementwise_min(__builtin_bit_cast(u16x2, (u32)r[0]), __builtin_bit_cast(u16x2, (u32)r[1]));
  const u32 z = ((u32)__ballot(t.x < t.y) >> 7) & 3u;
  st4c(rs, ag, ((v + 8) & ~7u) | z | (__ballot(m != 0xffffffffu) != 0ull ? 4u : 0u));
  vm_wait0();
}

__global__ void __launch_bounds__(256, 1) main_kernel(Params p) {
  const u32 bi = blockIdx.x & 255, rnd = blockIdx.x >> 16;
  u32 code = 0;
  if (PFI && (bi & PFI) == PFI)
    asm volatile("s_getpc_b64 s[40:41]\n\ts_and_b32 s40, s40, 0xffffff00\n\tglobal_load_dword %0, %1, s[40:41]" : "=v"(code) : "v"(threadIdx.x * 128) : "s40", "s41", "scc", "memory");
  const int x = rnd ? bi & 7 : __builtin_amdgcn_s_getreg(20 | (3 << 11)) & XCC_MASK, k = bi >> 3;
  const int ks = ((k >> 4) ^ (k >> LBIT)) & 1, b = (x < 2 ? k ^ (ks << 4) ^ (ks << LBIT) : k) * 8 + x;
  p.ws = ws_base(p.ws);
  const int w = __builtin_amdgcn_readfirstlane(threadIdx.x >> 6);
  const int q = qkva_id(b), hq = q * 171 >> 9;
  const rsrc_t rs = make_rsrc(p.ws, WS_USED);
  const bool lead = (bi | 4) == LEADER && w == 0;
  const u32 ag = OFF_GO + x * 128, ad = OFF_DONE + bi * 128;
  u32 go0, d, gl; u32x4 dl;
  {
    const u32x4 srd = make_srd(p.ws, WS_USED);
    const u32 lo = lead ? threadIdx.x * 128 : OOB;
    asm volatile("buffer_load_dword %0, off, %7, %8 sc1\n\tbuffer_load_dword %1, off, %7, %9 sc1\n\tbuffer_load_dword %2, %10, %7, %12 offen sc1\n\t"
                 "buffer_load_dword %3, %11, %7, %13 offen sc1\n\tbuffer_load_dword %4, %11, %7, %14 offen sc1\n\tbuffer_load_dword %5, %11, %7, %15 offen sc1\n\tbuffer_load_dword %6, %11, %7, %16 offen sc1"
                 : "=&v"(go0), "=&v"(d), "=&v"(gl), "=&v"(dl.x), "=&v"(dl.y), "=&v"(dl.z), "=&v"(dl.w)
                 : "s"(srd), "s"(ag), "s"(ad), "v"(threadIdx.x < 8 ? lo : OOB), "v"(lo), "s"(OFF_GO), "s"(OFF_DONE), "s"(OFF_DONE + 8 * KB), "s"(OFF_DONE + 16 * KB), "s"(OFF_DONE + 24 * KB) : "memory");
  }
  u32 rec = 0xffffffffu;
  if (x < 2 || q >= 0) early_loads(p, b, w);
  else if (x >= 4 && k < 31) rec = setup(p, k >= 15 ? NPROBE + (k - 15) * 4 + x - 4 : x == 6 ? k + 66 : k < 8 ? k : k + 73);
  else { if ((bi | 4) != LEADER) __builtin_amdgcn_s_sleep(127); vm_wait0(); wg_barrier(); }
  asm volatile("" : "+v"(go0), "+v"(d), "+v"(gl), "+v"(dl), "+v"(code));
  go0 = __builtin_amdgcn_readfirstlane(go0);
  d = (__builtin_amdgcn_readfirstlane(d) & ~15u) + 16 + rnd * 8 + x;
  st8c(rs, threadIdx.x ? OOB : ad, (u32x2){d, rec});
  if (lead) leader(p, dl, gl, rnd, x < 4);
  const u64 ka = (u64)__builtin_amdgcn_kernarg_segment_ptr();
  const u32x8 go = {go0, ag, w ? OOB : ad, d, (u32)ka, (u32)(ka >> 32), bi + 0x10000u, b < 128 ? 13u : 12u};
  if (x < 2) chain_role(p, w, b >> 7, chain_id(b) & 31, go);
  if (q >= 0) qkva_role(p, w, hq, q - 3 * hq, go);
  if ((x | 1) == 5 && k >= 16) opj_role(p, w, x - 4, (k >> 1) & 7, k & 1, go);
  if (x == 6 && k >= 10) ffn_role(p, w, k >= 10 + NFC, k >= 10 + NFC ? k - 10 - NFC : k - 10, go);
  u32 fb;
  for (u32 spins = 1;; ++spins) {
    fb = __builtin_amdgcn_readfirstlane(ld4c(rs, ag));
    if (fb != go0) break;
    if (spins > SPIN_CAP) return;
    if ((spins & 7) == 0) st8c(rs, threadIdx.x ? OOB : ad, (u32x2){d += 16, rec});
    asm volatile("s_sleep 4" ::: "memory");
  }
  if (fb & 4) asm volatile("s_waitcnt vmcnt(0)\n\ts_barrier\n\ts_branch " KERNEL_SYM :: "{s[0:1]}"(ka), "{s2}"(go[6]), "{v0}"(threadIdx.x) : "memory");
  fb = (fb & 1) * 8 * KB;
  if (x < 7) return;
#define RHELP 2
  if (k & 8) red_role<1>(p, w, k >> 4, k & 7, fb, 0, NSEG);
  else {
#pragma unroll 1
    for (int l = k >> 4; l < 2; ++l) red_role<2>(p, w, l, k & 7, fb, l > (k >> 4) ? NSEG - RHELP : 0, k >> 4 ? NSEG - RHELP : NSEG);
  }
}

}

extern "C" void kernel_launch(void* const* d_in, const int* in_sizes, int n_in, void* d_out, int out_size, void* d_ws,
                              size_t ws_size, hipStream_t stream) {
  using namespace rmt;
  Params p;
  p.x = (const bf16*)d_in[0]; p.mem0 = (const bf16*)d_in[1]; p.wq = (const bf16*)d_in[2]; p.wk = (const bf16*)d_in[3];
  p.wv = (const bf16*)d_in[4]; p.wo = (const bf16*)d_in[5];
  p.w1 = (const bf16*)d_in[8]; p.b1 = (const bf16*)d_in[9]; p.w2 = (const bf16*)d_in[10]; p.b2 = (const bf16*)d_in[11];
  p.out = (bf16*)d_out; p.ws = (char*)d_ws;
  (void)hipFuncSetAttribute((const void*)main_kernel, hipFuncAttributeMaxDynamicSharedMemorySize, LDS_BYTES);
  main_kernel<<<256, 256, LDS_BYTES, stream>>>(p);
}
```
